```python
import torch

def _softmax(x, axis=-1):
    return torch.softmax(x, dim=axis)
def _gelu(x):
    return torch.nn.functional.gelu(x, approximate="tanh")
def _silu(x):
    return torch.nn.functional.silu(x)
def _sigmoid(x):
    return torch.sigmoid(x)
def _layernorm(x, axis=-1, eps=1e-5):
    m = x.mean(dim=axis, keepdim=True)
    v = x.var(dim=axis, keepdim=True, unbiased=False)
    return (x - m) / torch.sqrt(v + eps)
def _scatter_add(tgt, idx, src, axis=0):
    return tgt.clone().index_add_(axis, idx, src)
def _segment_sum(src, seg, n):
    out = torch.zeros((n,) + src.shape[1:], dtype=src.dtype)
    return out.index_add_(0, seg, src)
def _pad(x, pw):
    flat = tuple(v for pair in reversed(pw) for v in pair)
    return torch.nn.functional.pad(x, flat)

kernel_name = 'gated_mix__compressed_kv_cla__dense_activation_gelu__decode'

def setup_inputs():

    _w = (lambda a: (lambda t: t.to(torch.bfloat16) if t.is_floating_point() else t)(torch.as_tensor(a)))
    def _A_setup():
        B, H, Sq, Sk, D, M = 2, 32, 1, 4096, 128, 576
        Dc = 4 * D
        return {
            "x": _w((lambda *s: torch.randn(*s, dtype=torch.bfloat16) * 0.05)(B, Sq, M)),
            "c_kv": _w((lambda *s: torch.randn(*s, dtype=torch.bfloat16) * 0.8)(B, Sk, Dc)),
            "w_q": _w((lambda *s: torch.randn(*s, dtype=torch.bfloat16) * 6.4)(M, H, D) * (M ** -0.5)),
            "w_uk": _w((lambda *s: torch.randn(*s, dtype=torch.bfloat16) * 6.4)(Dc, H, D) * (Dc ** -0.5)),
            "w_uv": _w((lambda *s: torch.randn(*s, dtype=torch.bfloat16) * 0.8)(Dc, H, D) * (Dc ** -0.5)),
            "w_o": _w((lambda *s: torch.randn(*s, dtype=torch.bfloat16) * 0.8)(H, D, M) * ((H * D) ** -0.5)),
        }
    _A_in = _A_setup()

    def _B_setup():
        B, S, D, F = 2, 1, 576, 4608
        return {
            "x": _w((lambda *s: torch.randn(*s, dtype=torch.bfloat16) * 0.05)(B, S, D)),
            "w1": _w((lambda *s: torch.randn(*s, dtype=torch.bfloat16) * 3.2)(D, F) * (D ** -0.5)),
            "b1": _w((lambda *s: torch.randn(*s, dtype=torch.bfloat16) * 3.2)(F) * 0.01),
            "w2": _w((lambda *s: torch.randn(*s, dtype=torch.bfloat16) * 0.8)(F, D) * (F ** -0.5)),
            "b2": _w((lambda *s: torch.randn(*s, dtype=torch.bfloat16) * 1.6)(D) * 0.01),
        }
    _B_in = _B_setup()

    return {
        "A_x": _A_in["x"],
        "A_c_kv": _A_in["c_kv"],
        "A_w_q": _A_in["w_q"],
        "A_w_uk": _A_in["w_uk"],
        "A_w_uv": _A_in["w_uv"],
        "A_w_o": _A_in["w_o"],
        "B_x": _B_in["x"],
        "B_w1": _B_in["w1"],
        "B_b1": _B_in["b1"],
        "B_w2": _B_in["w2"],
        "B_b2": _B_in["b2"],
        "gate_w": _w((lambda *s: torch.randn(*s, dtype=torch.bfloat16) * 0.05)(576) * 16.0),
    }

def reference(A_x, A_c_kv, A_w_q, A_w_uk, A_w_uv, A_w_o, B_x, B_w1, B_b1, B_w2, B_b2, gate_w):
    def _A_ref(x, c_kv, w_q, w_uk, w_uv, w_o):
        B, Sq, M = x.shape[0], x.shape[1], x.shape[2]
        Sk = c_kv.shape[1]
        H, D = w_q.shape[1], w_q.shape[2]
        q = torch.einsum("bqm,mhd->bhqd", x, w_q)
        k = torch.einsum("bkc,chd->bhkd", c_kv, w_uk)
        v = torch.einsum("bkc,chd->bhkd", c_kv, w_uv)
        scores = torch.einsum("bhqd,bhkd->bhqk", q, k) * (D ** -0.5)
        off = Sk - Sq
        qi = torch.arange(Sq)[:, None]
        kj = torch.arange(Sk)[None, :]
        cmask = kj <= (qi + off)
        scores = torch.where(cmask[None, None], scores, -1e9)
        p = _softmax(scores, axis=-1)
        ctx = torch.einsum("bhqk,bhkd->bhqd", p, v)
        return torch.einsum("bhqd,hdm->bqm", ctx, w_o)

    def _B_ref(x, w1, b1, w2, b2):
        h = torch.einsum("bsd,df->bsf", x, w1) + b1
        h = _gelu(h)
        return torch.einsum("bsf,fd->bsd", h, w2) + b2

    x = A_x
    g = _sigmoid((x * gate_w).sum(-1, keepdims=True))
    a = _A_ref(x, A_c_kv, A_w_q, A_w_uk, A_w_uv, A_w_o)
    b = _B_ref(x, B_w1, B_b1, B_w2, B_b2)
    return g * a + (1 - g) * b
```

```cpp
#include <hip/hip_runtime.h>
#include <hip/hip_bf16.h>
typedef unsigned short u16;
__device__ __forceinline__ float bf2f(u16 b){ return __uint_as_float(((unsigned)b)<<16); }
__device__ __forceinline__ u16 f2bf(float f){ unsigned u=__float_as_uint(f); u += 0x7FFFu + ((u>>16)&1u); return (u16)(u>>16);}
constexpr int B=2,H=32,SK=4096,D=128,M=576,DC=512,F=4608;
__global__ void k_q(const u16* x,const u16* wq,float* q){
  int n=blockIdx.x*256+threadIdx.x;
  float a0=0,a1=0;
  for(int m=0;m<M;m++){ float w=bf2f(wq[m*4096+n]); a0+=bf2f(x[m])*w; a1+=bf2f(x[M+m])*w;}
  q[n]=bf2f(f2bf(a0)); q[4096+n]=bf2f(f2bf(a1));
}
__global__ void k_qabs(const float* q,const u16* wuk,float* qa){
  int i=blockIdx.x*256+threadIdx.x;
  int h=i/512,c=i%512;
  float a0=0,a1=0;
  for(int d=0;d<D;d++){ float w=bf2f(wuk[c*4096+h*128+d]); a0+=q[h*128+d]*w; a1+=q[4096+h*128+d]*w;}
  qa[i]=a0; qa[16384+i]=a1;
}
__global__ void k_scores(const float* qa,const u16* ckv,float* s){
  int i=blockIdx.x*256+threadIdx.x;
  int k=i%SK, bh=i/SK, b=bh/H;
  float a=0;
  for(int c=0;c<DC;c++) a+=qa[bh*512+c]*bf2f(ckv[(b*SK+k)*DC+c]);
  s[i]=a*0.08838834764831845f;
}
__global__ void k_softmax(float* s){
  __shared__ float red[256];
  int bh=blockIdx.x; float* p=s+bh*SK; int t=threadIdx.x;
  float m=-1e30f; for(int k=t;k<SK;k+=256) m=fmaxf(m,p[k]);
  red[t]=m; __syncthreads(); for(int o=128;o>0;o>>=1){ if(t<o) red[t]=fmaxf(red[t],red[t+o]); __syncthreads();}
  m=red[0]; __syncthreads();
  float l=0; for(int k=t;k<SK;k+=256){ float e=__expf(p[k]-m); p[k]=e; l+=e;}
  red[t]=l; __syncthreads(); for(int o=128;o>0;o>>=1){ if(t<o) red[t]+=red[t+o]; __syncthreads();}
  l=red[0]; float r=1.f/l;
  for(int k=t;k<SK;k+=256) p[k]*=r;
}
__global__ void k_pv(const float* p,const u16* ckv,float* cc){
  int i=blockIdx.x*256+threadIdx.x;
  int c=i%512,bh=i/512,b=bh/H;
  float a=0;
  for(int k=0;k<SK;k++) a+=p[bh*SK+k]*bf2f(ckv[(b*SK+k)*DC+c]);
  cc[i]=a;
}
__global__ void k_uv(const float* cc,const u16* wuv,float* ctx){
  int n=blockIdx.x*256+threadIdx.x; int h=n/128;
  float a0=0,a1=0;
  for(int c=0;c<DC;c++){ float w=bf2f(wuv[c*4096+n]); a0+=cc[h*512+c]*w; a1+=cc[16384+h*512+c]*w;}
  ctx[n]=a0; ctx[4096+n]=a1;
}
__global__ void k_h(const u16* x,const u16* w1,const u16* b1,float* hh){
  int n=blockIdx.x*256+threadIdx.x;
  float a0=0,a1=0;
  for(int m=0;m<M;m++){ float w=bf2f(w1[m*F+n]); a0+=bf2f(x[m])*w; a1+=bf2f(x[M+m])*w;}
  float bb=bf2f(b1[n]);
  a0+=bb;a1+=bb;
  auto g=[](float v){ float u=0.7978845608028654f*(v+0.044715f*v*v*v); return 0.5f*v*(1.f+tanhf(u));};
  hh[n]=g(a0); hh[F+n]=g(a1);
}
__global__ void k_out(const u16* x,const u16* gw,const float* ctx,const u16* wo,const float* hh,const u16* w2,const u16* b2,u16* out){
  int i=blockIdx.x*64+threadIdx.x;
  int b=i/M,m=i%M;
  float g=0; for(int j=0;j<M;j++) g+=bf2f(x[b*M+j])*bf2f(gw[j]);
  g=1.f/(1.f+__expf(-g));
  float a=0; for(int j=0;j<4096;j++) a+=ctx[b*4096+j]*bf2f(wo[j*M+m]);
  float bb=0; for(int j=0;j<F;j++) bb+=hh[b*F+j]*bf2f(w2[j*M+m]);
  bb+=bf2f(b2[m]);
  out[i]=f2bf(g*a+(1.f-g)*bb);
}
extern "C" void kernel_launch(void* const* d_in, const int* in_sizes, int n_in,
                              void* d_out, int out_size, void* d_ws, size_t ws_size,
                              hipStream_t stream) {
  const u16* x=(const u16*)d_in[0]; const u16* ckv=(const u16*)d_in[1]; const u16* wq=(const u16*)d_in[2];
  const u16* wuk=(const u16*)d_in[3]; const u16* wuv=(const u16*)d_in[4]; const u16* wo=(const u16*)d_in[5];
  const u16* w1=(const u16*)d_in[7]; const u16* b1=(const u16*)d_in[8]; const u16* w2=(const u16*)d_in[9]; const u16* b2=(const u16*)d_in[10];
  const u16* gw=(const u16*)d_in[11];
  float* ws=(float*)d_ws;
  float* q=ws; float* qa=q+8192; float* s=qa+32768; float* cc=s+2*32*4096; float* ctx=cc+32768; float* hh=ctx+8192;
  k_q<<<16,256,0,stream>>>(x,wq,q);
  k_qabs<<<64,256,0,stream>>>(q,wuk,qa);
  k_scores<<<1024,256,0,stream>>>(qa,ckv,s);
  k_softmax<<<64,256,0,stream>>>(s);
  k_pv<<<128,256,0,stream>>>(s,ckv,cc);
  k_uv<<<16,256,0,stream>>>(cc,wuv,ctx);
  k_h<<<18,256,0,stream>>>(x,w1,b1,hh);
  k_out<<<18,64,0,stream>>>(x,gw,ctx,wo,hh,w2,b2,(u16*)d_out);
}
```

```cpp
#include <hip/hip_runtime.h>
#include <stdint.h>

typedef unsigned u32;
typedef unsigned long long u64;
typedef unsigned short u16;
typedef u32 v4u __attribute__((ext_vector_type(4)));
typedef u32 v3u __attribute__((ext_vector_type(3)));
typedef u32 v2u __attribute__((ext_vector_type(2)));
typedef float v4f __attribute__((ext_vector_type(4)));
typedef float v2f __attribute__((ext_vector_type(2)));
typedef short v8s __attribute__((ext_vector_type(8)));
typedef short v4s __attribute__((ext_vector_type(4)));
typedef __bf16 v2b __attribute__((ext_vector_type(2)));
#define LAS __attribute__((address_space(3)))
#define GAS __attribute__((address_space(1)))
#define CAS __attribute__((address_space(4)))
#define DI __device__ __forceinline__

constexpr u32 WS_E = 0;
constexpr u32 WS_QP = 4096;
constexpr u32 WS_QG = WS_QP + 32 * 8 * 1024;
constexpr u32 WS_ST = WS_QG + 2 * 32 * 2048;
constexpr u32 WS_SL = WS_ST + 128 * 128;
constexpr u32 WS_FG = WS_SL + 128 * 128;
constexpr u32 WS_OG = WS_FG + 72 * 256 * 64;
constexpr u32 WS_END = WS_OG + 32 * 4 * 64 * 512;
constexpr u32 WS_DBG = 8u << 20;
constexpr u32 L1_Q = 131072, L1_MX = L1_Q + 16384, L1_P = L1_MX + 512, L1_X = L1_P + 4352, L1_HP = L1_X + 3072, L1_HB = L1_HP + 2304;
constexpr u32 L1_W1 = 0, L1_W2 = 46080;
constexpr u32 L2_UV = 114688, L2_UK = 81920;
constexpr u32 L2_BQ = 147456, L2_BC = L2_BQ + 4352, L2_CT = L2_BC + 4352, L2_F = L2_CT + 544, L2_IL = L2_F + 256, L2_RD = L2_IL + 16, L2_X = L2_RD + 512, L2_GW = L2_X + 3072;
static_assert(L1_HB + 256 <= 163840 && L2_GW + 2048 <= 163840, "lds");
static_assert(L1_X % 16 == 0 && L2_X % 16 == 0 && L2_GW % 16 == 0 && L2_RD % 16 == 0, "align");
constexpr int SC1 = 16, NT = 2;
constexpr u32 SPIN_CAP = 1u << 15;

#define STAMP(i) do {} while (0)

DI v4u mk_srd(const void* p, u32 bytes) {
  const u64 a = (u64)p;
  return v4u{(u32)a, (u32)(a >> 32) & 0xffffu, bytes, 0x00020000u};
}
DI __amdgpu_buffer_rsrc_t mk_rsrc(const void* p, u32 bytes) { return __builtin_amdgcn_make_buffer_rsrc((void*)p, 0, (int)bytes, 0x00020000); }
DI void dma(v4u srd, u32 voff, u32 soff, u32 lds) {
  asm volatile("s_mov_b32 m0, %3\n\ts_nop 0\n\tbuffer_load_dwordx4 %0, %1, %2 offen nt lds" ::"v"(voff), "s"(srd), "s"(soff), "s"(lds) : "memory");
}
#define BAR() asm volatile("s_waitcnt lgkmcnt(0)\n\ts_barrier" ::: "memory")
#define VMW(n) asm volatile("s_waitcnt vmcnt(" #n ")" ::: "memory")
#define ORDER() asm volatile("" ::: "memory")
DI v4f mfma(v4u a, v4u b, v4f c) { return __builtin_amdgcn_mfma_f32_16x16x32_bf16(__builtin_bit_cast(v8s, a), __builtin_bit_cast(v8s, b), c, 0, 0, 0); }
DI u32 pk(float a, float b) { return __builtin_bit_cast(u32, __builtin_convertvector(v2f{a, b}, v2b)); }
DI float bflo(u32 x) { return __builtin_bit_cast(float, x << 16); }
DI float bfhi(u32 x) { return __builtin_bit_cast(float, x & 0xffff0000u); }
DI float f_(u32 x) { return __builtin_bit_cast(float, x); }
DI u32 u_(float x) { return __builtin_bit_cast(u32, x); }
template <int C> DI float dpp(float x) { return f_((u32)__builtin_amdgcn_update_dpp(0, (int)u_(x), C, 0xf, 0xf, true)); }
DI float sum8(float x) { x += dpp<0xB1>(x); x += dpp<0x4E>(x); x += dpp<0x141>(x); return x; }
DI float add16(float x) { auto r = __builtin_amdgcn_permlane16_swap(u_(x), u_(x), false, false); return f_(r[0]) + f_(r[1]); }
DI float add32(float x) { auto r = __builtin_amdgcn_permlane32_swap(u_(x), u_(x), false, false); return f_(r[0]) + f_(r[1]); }
DI float max16(float x) { auto r = __builtin_amdgcn_permlane16_swap(u_(x), u_(x), false, false); return fmaxf(f_(r[0]), f_(r[1])); }
DI float max32(float x) { auto r = __builtin_amdgcn_permlane32_swap(u_(x), u_(x), false, false); return fmaxf(f_(r[0]), f_(r[1])); }
DI v4u lds128(LAS char* L, u32 a) { return *(LAS v4u*)(L + a); }
DI v2u ldstr(LAS char* L, u32 a) { return __builtin_bit_cast(v2u, __builtin_amdgcn_ds_read_tr16_b64_v4i16((LAS v4s*)(L + a))); }
DI v4u cat(v2u a, v2u b) { return v4u{a.x, a.y, b.x, b.y}; }
DI bool tagok(v4u a, u32 tag) { return (a.y == tag) & (a.w == tag); }
DI v4u blkfrag(LAS char* L, u32 blk, u32 t, u32 lane) {
  const u32 g = lane >> 4, q = (lane >> 2) & 3, p = lane & 3;
  const u32 r0 = 4 * (g & 1) + q, r1 = r0 ^ 4, pc = 2 * t + (p >> 1);
  return cat(ldstr(L, blk + r0 * 128 + ((pc ^ (r0 & 6)) << 4) + 8 * (p & 1)), ldstr(L, blk + r1 * 128 + ((pc ^ (r1 & 6)) << 4) + 8 * (p & 1)));
}

__global__ void __launch_bounds__(512, 2) fused_decode(const u16* __restrict__ x, const u16* __restrict__ ckv, const u16* __restrict__ wq,
    const u16* __restrict__ wuk, const u16* __restrict__ wuv, const u16* __restrict__ wo, const u16* __restrict__ w1, const u16* __restrict__ b1,
    const u16* __restrict__ w2, const u16* __restrict__ b2, const u16* __restrict__ gw, u16* __restrict__ out, char* ws) {
  __shared__ __attribute__((aligned(1024))) char smem[163840];
  LAS char* L = (LAS char*)smem;
  const u32 tid = threadIdx.x, lane = tid & 63, w = __builtin_amdgcn_readfirstlane(tid >> 6);
  const u32 blk = blockIdx.x, xc = blk & 7, ii = blk >> 3;
  const u32 lr = lane & 15, lg = lane >> 4;
  STAMP(0);
  const __amdgpu_buffer_rsrc_t rws = mk_rsrc(ws, WS_END);
  const u32 tag = *(const CAS u32*)(ws + WS_E) + 1u;
  const bool g2role = ii < 16;
  const u32 k = ii & 15;

  const u32 ha = 4 * xc + (ii >> 3), jm = ii & 7;
  const u32 h = 4 * xc + (k >> 2), j = k & 3;
  const u32 g1 = 16 * xc + k, hg = g1 & 1, tile = g1 >> 1, b = tile >> 5;
  const v4u sck = mk_srd(ckv, 2 * 4096 * 1024);
  auto ck_dma = [&](int i) {
    const u32 row = 16 * w + (i ^ (8 * hg));
    dma(sck, (lane * 16) ^ (hg * 32) ^ (u32)((((i & 3) << 2) | (i >> 2)) << 4), (tile * 128 + row) * 1024, row * 1024);
  };
  if (w == 0) {
    v4u wqv[18];
    v3u xs[3];
    const __amdgpu_buffer_rsrc_t rx = mk_rsrc(x, 2304), rq = mk_rsrc(wq, 576 * 8192);
    const u32 xo = ((lr >> 3) * 576 + 72 * jm + 18 * lg) * 2;
#pragma unroll
    for (int i = 0; i < 3; ++i) xs[i] = __builtin_amdgcn_raw_buffer_load_b96(rx, xo + 12 * i, 0, 0);
    const u32 vo = (72 * jm + 18 * lg) * 8192 + ha * 256 + lr * 16;
#pragma unroll
    for (int s = 0; s < 18; ++s) wqv[s] = __builtin_amdgcn_raw_buffer_load_b128(rq, vo, s * 8192, NT);
    BAR();
    const u32 di = (lr & 7) >> 1, sh = (lr & 1) * 16;
    u32 se[4], so[4];
#pragma unroll
    for (int i = 0; i < 4; ++i) {
      se[i] = di == (u32)i ? ((0x0100u << sh) | (0x0c0c0000u >> sh)) : 0x0c0c0c0cu;
      so[i] = di == (u32)i ? ((0x0302u << sh) | (0x0c0c0000u >> sh)) : 0x0c0c0c0cu;
    }
    v4f acc = {0.f, 0.f, 0.f, 0.f};
#pragma unroll
    for (int s = 0; s < 18; ++s) {
      const u32 xv = xs[s / 6][(s % 6) >> 1];
      v4u a;
#pragma unroll
      for (int i = 0; i < 4; ++i) a[i] = __builtin_amdgcn_perm(0u, xv, (s & 1) ? so[i] : se[i]);
      acc = mfma(a, wqv[s], acc);
    }
    __builtin_amdgcn_raw_buffer_store_b128(v4u{pk(acc[0], acc[1]), tag, pk(acc[2], acc[3]), tag}, rws, WS_QP + (ha * 8 + jm) * 1024 + lane * 16, 0, SC1);
  } else {
    BAR();
    if (w == 7) {
      const v4u sx = mk_srd(x, 2304);
      const u32 lx = g2role ? L2_X : L1_X;
      dma(sx, lane * 16, 0, lx); dma(sx, lane * 16, 1024, lx + 1024); dma(sx, lane * 16, 2048, lx + 2048);
    }
    if (g2role) {
      const v4u suk = mk_srd(wuk, 512 * 8192);
#pragma nounroll
      for (u32 tw = w == 1 ? 0 : w; tw <= w; ++tw) {
#pragma unroll
        for (int u = 0; u < 4; ++u) {
          const u32 row = 4 * u + lg;
          dma(suk, row * 8192 + ((lr ^ row) << 4), (128 * j + 16 * tw) * 8192 + h * 256, L2_UK + tw * 4096 + u * 1024);
        }
      }
      if (w == 6) {
        const v4u sg = mk_srd(gw, 1152);
        dma(sg, lane * 16, 0, L2_GW); dma(sg, lane * 16, 1024, L2_GW + 1024);
      }
    } else {
#pragma unroll
      for (int i = 0; i < 3; ++i) ck_dma(i);
    }
  }
  STAMP(1);
  BAR();

  if (g2role) {
    const bool reducer = k < 9;
    const u32 rho = 9 * xc + k, rmt = rho >> 1, rb_ = rho & 1;
    u32 b2v = 0;
    if (reducer && tid < 16) b2v = b2[16 * rmt + tid];
    __builtin_amdgcn_s_sleep(6);
    v4u qp;
#pragma nounroll
    for (u32 sp = 0; sp < SPIN_CAP; ++sp) {
      ORDER();
      qp = __builtin_amdgcn_raw_buffer_load_b128(rws, WS_QP + (h * 8 + w) * 1024 + lane * 16, 0, SC1);
      if (__all(tagok(qp, tag))) break;
    }
    asm volatile("" : "+v"(b2v));
    VMW(0);
    STAMP(2);
    *(LAS v2u*)(L + L2_BQ + ((lg >> 1) * 8 + w) * 272 + (8 * lr + 4 * (lg & 1)) * 2) = v2u{qp.x, qp.z};
    BAR();
    {
      v4f acc = {0.f, 0.f, 0.f, 0.f};
#pragma unroll
      for (int s = 0; s < 4; ++s)
        acc = mfma(lds128(L, L2_UK + w * 4096 + lr * 256 + (((4 * s + lg) ^ lr) << 4)), lds128(L, L2_BQ + lr * 272 + (32 * s + 8 * lg) * 2), acc);
      const float sc = 0.08838834764831845f * 1.4426950408889634f;
      const float v0 = sum8(acc[0]) * sc, v1 = sum8(acc[1]) * sc, v2 = sum8(acc[2]) * sc, v3 = sum8(acc[3]) * sc;
      if ((lane & 7) == 0)
        __builtin_amdgcn_raw_buffer_store_b128(v4u{pk(v0, v1), tag, pk(v2, v3), tag}, rws, WS_QG + ((lr >> 3) * 32 + h) * 2048 + (32 * j + 4 * w + lg) * 16, 0, SC1);
    }
    STAMP(3);
    BAR();
    const u32 rr = lane >> 3, pcs = ((lane & 7) ^ (rr & 6)) << 4;
    const v4u suv = mk_srd(wuv, 512 * 8192), swo = mk_srd(wo, 4096 * 1152);
#pragma unroll
    for (int n = 0; n < 4; ++n) {
      const u32 slot = 8 * n + w;
      dma(suv, rr * 8192 + pcs, (128 * j + 8 * (slot >> 1)) * 8192 + h * 256 + (slot & 1) * 128, L2_UV + slot * 1024);
    }
    auto wo_dma = [&](u32 slot) {
      const u32 rbl = (slot * 57) >> 9, cg = slot - 9 * rbl;
      dma(swo, rr * 1152 + pcs, (h * 128 + 8 * ((rbl + 4 * j) & 15)) * 1152 + cg * 128, slot * 1024);
    };
#pragma unroll
    for (int n = 0; n < 14; ++n) wo_dma(8 * n + w);
    VMW(14);
    BAR();
    v4u uvA[4];
#pragma unroll
    for (int s = 0; s < 4; ++s) uvA[s] = blkfrag(L, L2_UV + ((4 * s + lg) * 2 + (w >> 2)) * 1024, w & 3, lane);
    float gate = 0.f;
    if (reducer) {
      float a = 0.f;
#pragma unroll
      for (int i = 0; i < 5; ++i) {
        const u32 e = lane + 64 * i;
        if (e < 288) {
          const u32 xv = *(LAS u32*)(L + L2_X + rb_ * 1152 + e * 4), gv = *(LAS u32*)(L + L2_GW + e * 4);
          a += bflo(xv) * bflo(gv) + bfhi(xv) * bfhi(gv);
        }
      }
      a = sum8(a); a += dpp<0x140>(a); a = add32(add16(a));
      gate = 1.f / (1.f + __expf(-a));
    }
    BAR();
#pragma unroll
    for (int n = 14; n < 18; ++n) wo_dma(8 * n + w);
    STAMP(4);
    VMW(0);
    STAMP(5);
    float fch = 0.f;
    const u32 sto = (lane * 2 + (h >> 4)) * 128 + (h & 15) * 8;
    if (w == 0) {
      v2u mv;
#pragma nounroll
    for (u32 sp = 0; sp < SPIN_CAP; ++sp) {
        ORDER();
        mv = __builtin_amdgcn_raw_buffer_load_b64(rws, WS_ST + sto, 0, SC1);
        if (__all(mv.y == tag)) break;
      }
      float M = f_(mv.x);
      M = fmaxf(M, dpp<0xB1>(M)); M = fmaxf(M, dpp<0x4E>(M)); M = fmaxf(M, dpp<0x141>(M)); M = fmaxf(M, dpp<0x140>(M)); M = max16(M);
      fch = __builtin_amdgcn_exp2f(f_(mv.x) - M);
      const u32 hi = pk(fch, 0.f) & 0xffffu;
      *(LAS u32*)(L + L2_F + lane * 4) = pk(fch, fch - bflo(hi));
    }
    BAR();
    STAMP(6);
    const u32 ogo = WS_OG + ((h * 4 + j) * 64 + 8 * w + lg) * 512 + lr * 16;
    v4u og[4];
    v2u lv = {0u, tag};
#pragma unroll
    for (int e = 0; e < 4; ++e) og[e] = __builtin_amdgcn_raw_buffer_load_b128(rws, ogo + (e >> 1) * 2048 + (e & 1) * 256, 0, SC1);
    if (w == 0) lv = __builtin_amdgcn_raw_buffer_load_b64(rws, WS_SL + sto, 0, SC1);
    ORDER();
    v4u woA[5][4];
#pragma unroll
    for (int n = 0; n < 4; ++n)
#pragma unroll
      for (int s = 0; s < 4; ++s) woA[n][s] = blkfrag(L, ((4 * s + lg) * 9 + 2 * n + (w >> 2)) * 1024, w & 3, lane);
#pragma unroll
    for (int s = 0; s < 4; ++s) woA[4][s] = blkfrag(L, ((4 * s + lg) * 9 + 8) * 1024, w & 3, lane);
    v4u fA[2];
    {
      const u32 di = (lr & 7) >> 1, sh = (lr & 1) * 16, src = (lr >> 3) ? 0x0302u : 0x0100u;
#pragma unroll
      for (int e = 0; e < 2; ++e) {
        const u32 fv = *(LAS u32*)(L + L2_F + (8 * w + 4 * e + lg) * 4);
#pragma unroll
        for (int i = 0; i < 4; ++i) fA[e][i] = __builtin_amdgcn_perm(0u, fv, di == (u32)i ? ((src << sh) | (0x0c0c0000u >> sh)) : 0x0c0c0c0cu);
      }
    }
    ORDER();
#pragma nounroll
    for (u32 sp = 0; sp < SPIN_CAP; ++sp) {
      if (__all(tagok(og[0], tag) & tagok(og[1], tag) & tagok(og[2], tag) & tagok(og[3], tag) & (lv.y == tag))) break;
      ORDER();
#pragma unroll
      for (int e = 0; e < 4; ++e) og[e] = __builtin_amdgcn_raw_buffer_load_b128(rws, ogo + (e >> 1) * 2048 + (e & 1) * 256, 0, SC1);
      if (w == 0) lv = __builtin_amdgcn_raw_buffer_load_b64(rws, WS_SL + sto, 0, SC1);
    }
    STAMP(7);
    {
      v4f acc = {0.f, 0.f, 0.f, 0.f};
      acc = mfma(fA[0], v4u{og[0].x, og[0].z, og[1].x, og[1].z}, acc);
      acc = mfma(fA[1], v4u{og[2].x, og[2].z, og[3].x, og[3].z}, acc);
      const u32 U = 16 * (lg & 1) + lr;
      *(LAS v2u*)(L + L2_BC + ((w >> 2) * 8 + (w & 3) * 2 + (lg >> 1)) * 272 + (U ^ ((U >> 1) & 1)) * 8) = v2u{pk(acc[0], acc[1]), pk(acc[2], acc[3])};
      if (w == 0) {
        float ls = fch * f_(lv.x);
        ls = sum8(ls); ls += dpp<0x140>(ls); ls = add16(ls);
        if ((lane & 31) == 0) *(LAS float*)(L + L2_IL + (lane >> 5) * 4) = 1.f / ls;
      }
    }
    BAR();
    {
      v4f acc = {0.f, 0.f, 0.f, 0.f};
#pragma unroll
      for (int s = 0; s < 4; ++s) acc = mfma(uvA[s], lds128(L, L2_BC + lr * 272 + (32 * s + 8 * lg) * 2), acc);
      const float il = *(LAS float*)(L + L2_IL + (lr >> 3) * 4);
      const float v0 = sum8(acc[0]) * il, v1 = sum8(acc[1]) * il, v2 = sum8(acc[2]) * il, v3 = sum8(acc[3]) * il;
      const u32 U = 4 * w + lg;
      if ((lane & 7) == 0) *(LAS v2u*)(L + L2_CT + (lr >> 3) * 272 + (U ^ ((U >> 1) & 1)) * 8) = v2u{pk(v0, v1), pk(v2, v3)};
    }
    BAR();
    STAMP(8);
    {
      v4u cb[4];
#pragma unroll
      for (int s = 0; s < 4; ++s) cb[s] = lds128(L, L2_CT + (lr & 1) * 272 + (32 * ((s + j) & 3) + 8 * lg) * 2);
      const u32 fo = WS_FG + (lr * 256 + 128 + h * 4 + j) * 64 + lg * 16;
#pragma unroll
      for (int n = 0; n < 5; ++n) {
        if (n < 4 || w < 4) {
          v4f acc = {0.f, 0.f, 0.f, 0.f};
#pragma unroll
          for (int s = 0; s < 4; ++s) acc = mfma(woA[n][s], cb[s], acc);
          if (lr < 2) __builtin_amdgcn_raw_buffer_store_b128(v4u{pk(acc[0], acc[1]), tag, pk(acc[2], acc[3]), tag}, rws, fo, (w + 8 * n) * 2 * 256 * 64, SC1);
        }
      }
    }
    STAMP(9);
    if (reducer) {
      __builtin_amdgcn_s_sleep(10);
      const u32 fo = WS_FG + rho * 256 * 64 + tid * 16;
      v4u pm, pa;
#pragma nounroll
    for (u32 sp = 0; sp < SPIN_CAP; ++sp) {
        ORDER();
        pm = __builtin_amdgcn_raw_buffer_load_b128(rws, fo, 0, SC1);
        pa = __builtin_amdgcn_raw_buffer_load_b128(rws, fo + 8192, 0, SC1);
        if (__all(tagok(pm, tag) & tagok(pa, tag))) break;
      }
      STAMP(10);
      const float gm = 1.f - gate;
      v4f v = {gate * bflo(pa.x) + gm * bflo(pm.x), gate * bfhi(pa.x) + gm * bfhi(pm.x), gate * bflo(pa.z) + gm * bflo(pm.z), gate * bfhi(pa.z) + gm * bfhi(pm.z)};
#pragma unroll
      for (int i = 0; i < 4; ++i) {
        float t = v[i];
        t += dpp<0x124>(t); t += dpp<0x128>(t);
        v[i] = add32(add16(t));
      }
      if (lane < 4) *(LAS v4f*)(L + L2_RD + w * 64 + lane * 16) = v;
      BAR();
      if (tid < 16) {
        float s = gm * bflo(b2v);
#pragma unroll
        for (int i = 0; i < 8; ++i) s += *(LAS float*)(L + L2_RD + i * 64 + tid * 4);
        out[rb_ * 576 + 16 * rmt + tid] = (u16)pk(s, 0.f);
      }
      if (blk == 0 && tid == 0) __hip_atomic_store((GAS u32*)(ws + WS_E), tag, __ATOMIC_RELAXED, __HIP_MEMORY_SCOPE_AGENT);
      STAMP(11);
    }
  } else {
    u32 b1v = 0;
    if (tid < 128 && (tid & 63) < 36) b1v = b1[36 * g1 + (tid & 63)];
    if (w == 0) {
#pragma unroll
      for (int i = 0; i < 3; ++i) ck_dma(i);
    }
#pragma unroll
    for (int i = 3; i < 16; ++i) ck_dma(i);
    STAMP(2);
    VMW(0);
    asm volatile("" : "+v"(b1v));
    STAMP(3);
    const u32 qo = WS_QG + (b * 32 + hg * 16) * 2048 + tid * 16;
    v4u qg[4];
#pragma unroll
    for (int u = 0; u < 4; ++u) qg[u] = __builtin_amdgcn_raw_buffer_load_b128(rws, qo + u * 8192, 0, SC1);
    ORDER();
    const u32 sg = (0x9cu >> (2 * lg)) & 3;
    const u32 pir = ((lr & 3) << 2) | (lr >> 2);
    v4u kA[16];
#pragma unroll
    for (int s = 0; s < 16; ++s) kA[s] = lds128(L, (16 * w + lr) * 1024 + ((((4 * s) & 15) + sg) ^ pir) * 16 + (s >> 2) * 256);
    BAR();
    v4u vA[4][4];
    {
      const u32 q = (lane >> 2) & 3, p = lane & 3;
#pragma unroll
      for (int tt = 0; tt < 4; ++tt)
#pragma unroll
        for (int t = 0; t < 4; ++t) {
          v2u hv[2];
#pragma unroll
          for (int u = 0; u < 2; ++u)
            hv[u] = ldstr(L, (32 * t + 8 * lg + 4 * u + q) * 1024 + (((8 * w + 2 * tt + (p >> 1)) ^ ((q << 2) | (2 * (lg & 1) + u))) << 4) + 8 * (p & 1));
          vA[tt][t] = cat(hv[0], hv[1]);
        }
    }
    ORDER();
#pragma nounroll
    for (u32 sp = 0; sp < SPIN_CAP; ++sp) {
      if (__all(tagok(qg[0], tag) & tagok(qg[1], tag) & tagok(qg[2], tag) & tagok(qg[3], tag))) break;
      ORDER();
#pragma unroll
      for (int u = 0; u < 4; ++u) qg[u] = __builtin_amdgcn_raw_buffer_load_b128(rws, qo + u * 8192, 0, SC1);
    }
    STAMP(4);
#pragma unroll
    for (int u = 0; u < 4; ++u) {
      const u32 head = 4 * u + (w >> 1), c4 = tid & 127;
      *(LAS v2u*)(L + L1_Q + head * 1024 + (((c4 >> 1) ^ head) << 4) + (c4 & 1) * 8) = v2u{qg[u].x, qg[u].z};
    }
    BAR();
    const v4u sw1 = mk_srd(w1, 576 * 9216), sw2 = mk_srd(w2, 4608 * 1152);
    const u32 cb1 = (72 * g1) & ~15u, co1 = (72 * g1) & 15u;
    auto w1_dma = [&](u32 n) {
      const u32 P = 64 * n + lane, row = (P * 52429u) >> 18, pc = P - 5 * row;
      dma(sw1, row * 9216 + pc * 16, cb1, L1_W1 + n * 1024);
    };
#pragma unroll
    for (int n = 0; n < 5; ++n) w1_dma(8 * n + w);
    v4f S = {0.f, 0.f, 0.f, 0.f};
#pragma unroll
    for (int s = 0; s < 16; ++s) S = mfma(kA[s], lds128(L, L1_Q + lr * 1024 + ((((4 * s) & 15) + sg) ^ lr) * 16 + (s >> 2) * 256), S);
    {
      const float mw = max32(max16(fmaxf(fmaxf(S[0], S[1]), fmaxf(S[2], S[3]))));
      if (lane < 16) *(LAS float*)(L + L1_MX + lr * 32 + w * 4) = mw;
    }
    BAR();
    STAMP(5);
    {
      const v4f m0 = *(LAS v4f*)(L + L1_MX + lr * 32), m1 = *(LAS v4f*)(L + L1_MX + lr * 32 + 16);
      const float M = fmaxf(fmaxf(fmaxf(m0[0], m0[1]), fmaxf(m0[2], m0[3])), fmaxf(fmaxf(m1[0], m1[1]), fmaxf(m1[2], m1[3])));
      if (tid < 16) __builtin_amdgcn_raw_buffer_store_b64(v2u{u_(M), tag}, rws, WS_ST + g1 * 128 + lr * 8, 0, SC1);
      const float p0 = __builtin_amdgcn_exp2f(S[0] - M), p1 = __builtin_amdgcn_exp2f(S[1] - M), p2 = __builtin_amdgcn_exp2f(S[2] - M), p3 = __builtin_amdgcn_exp2f(S[3] - M);
      *(LAS v2u*)(L + L1_P + lr * 272 + (16 * w + 4 * lg) * 2) = v2u{pk(p0, p1), pk(p2, p3)};
    }
    BAR();
    {
      v4u pB[4];
#pragma unroll
      for (int t = 0; t < 4; ++t) pB[t] = lds128(L, L1_P + lr * 272 + (32 * t + 8 * lg) * 2);
      const u32 oo = WS_OG + (((hg * 16 + lr) * 4 + (w >> 1)) * 64 + tile) * 512 + (w & 1) * 256 + lg * 16;
#pragma unroll
      for (int tt = 0; tt < 4; ++tt) {
        v4f acc = {0.f, 0.f, 0.f, 0.f};
#pragma unroll
        for (int t = 0; t < 4; ++t) acc = mfma(vA[tt][t], pB[t], acc);
        __builtin_amdgcn_raw_buffer_store_b128(v4u{pk(acc[0], acc[1]), tag, pk(acc[2], acc[3]), tag}, rws, oo + tt * 64, 0, SC1);
      }
      if (w == 7) {
        v4f acc = {0.f, 0.f, 0.f, 0.f};
        const v4u ones = {0x3f803f80u, 0x3f803f80u, 0x3f803f80u, 0x3f803f80u};
#pragma unroll
        for (int t = 0; t < 4; ++t) acc = mfma(ones, pB[t], acc);
        if (lane < 16) __builtin_amdgcn_raw_buffer_store_b64(v2u{u_(acc[0]), tag}, rws, WS_SL + g1 * 128 + lr * 8, 0, SC1);
      }
    }
    STAMP(6);
    if (w < 5) w1_dma(40 + w);
    const u32 rr = lane >> 3, pcs = ((lane & 7) ^ (rr & 6)) << 4;
    auto w2_dma = [&](u32 slot) {
      const u32 rbl = (slot * 57) >> 9, cg = slot - 9 * rbl, row = min(8 * rbl + rr, 35u);
      dma(sw2, row * 1152 + pcs, 36 * g1 * 1152 + cg * 128, L1_W2 + slot * 1024);
    };
#pragma unroll
    for (int n = 0; n < 5; ++n) w2_dma(8 * n + w);
    if (w < 5) { w2_dma(40 + w); VMW(6); } else { VMW(5); }
    BAR();
    STAMP(7);
    if (w < 6) {
      const u32 q = (lane >> 2) & 3, p = lane & 3;
      v4f acc[3] = {{0.f, 0.f, 0.f, 0.f}, {0.f, 0.f, 0.f, 0.f}, {0.f, 0.f, 0.f, 0.f}};
#pragma unroll
      for (int ss = 0; ss < 3; ++ss) {
        const u32 s = 3 * w + ss;
        const v4u xb = lds128(L, L1_X + (lr & 1) * 1152 + (32 * s + 8 * lg) * 2);
        const u32 a0 = L1_W1 + (32 * s + 8 * lg + q) * 80 + co1 + p * 8;
#pragma unroll
        for (int ft = 0; ft < 3; ++ft) acc[ft] = mfma(cat(ldstr(L, a0 + ft * 32), ldstr(L, a0 + 320 + ft * 32)), xb, acc[ft]);
      }
      if (lr < 2) {
#pragma unroll
        for (int ft = 0; ft < 3; ++ft) *(LAS v4f*)(L + L1_HP + ((w * 2 + lr) * 48 + 16 * ft + 4 * lg) * 4) = acc[ft];
      }
    }
    BAR();
    if (tid < 128) {
      const u32 f = tid & 63, bb = tid >> 6;
      float hv = 0.f;
      if (f < 36) {
        float v = bflo(b1v);
#pragma unroll
        for (int i = 0; i < 6; ++i) v += *(LAS float*)(L + L1_HP + ((i * 2 + bb) * 48 + f) * 4);
        const float u2 = 1.5957691216057308f * (v + 0.044715f * v * v * v);
        hv = v / (1.f + __expf(-u2));
      }
      const u32 U = f >> 2;
      *(LAS u16*)(L + L1_HB + bb * 128 + (U ^ ((U >> 1) & 1)) * 8 + (f & 3) * 2) = (u16)pk(hv, 0.f);
    }
    VMW(0);
    BAR();
    STAMP(8);
    {
      const v4u h0 = lds128(L, L1_HB + (lr & 1) * 128 + 16 * lg), h1 = lds128(L, L1_HB + (lr & 1) * 128 + 64 + 16 * lg);
      const u32 fo = WS_FG + (lr * 256 + g1) * 64 + lg * 16;
#pragma unroll
      for (int n = 0; n < 5; ++n) {
        if (n < 4 || w < 4) {
          const u32 cg = n < 4 ? 2 * n + (w >> 2) : 8;
          v4f acc = {0.f, 0.f, 0.f, 0.f};
          acc = mfma(blkfrag(L, L1_W2 + (lg * 9 + cg) * 1024, w & 3, lane), h0, acc);
          acc = mfma(blkfrag(L, L1_W2 + (36 + cg) * 1024, w & 3, lane), h1, acc);
          if (lr < 2) __builtin_amdgcn_raw_buffer_store_b128(v4u{pk(acc[0], acc[1]), tag, pk(acc[2], acc[3]), tag}, rws, fo, (w + 8 * n) * 2 * 256 * 64, SC1);
        }
      }
    }
    STAMP(9);
  }
}

extern "C" void kernel_launch(void* const* d_in, const int* in_sizes, int n_in, void* d_out, int out_size, void* d_ws, size_t ws_size, hipStream_t stream) {
  fused_decode<<<256, 512, 0, stream>>>((const u16*)d_in[0], (const u16*)d_in[1], (const u16*)d_in[2], (const u16*)d_in[3], (const u16*)d_in[4],
      (const u16*)d_in[5], (const u16*)d_in[7], (const u16*)d_in[8], (const u16*)d_in[9], (const u16*)d_in[10], (const u16*)d_in[11], (u16*)d_out, (char*)d_ws);
}
```

```cpp
#include <hip/hip_runtime.h>
#include <stdint.h>

typedef unsigned u32;
typedef unsigned long long u64;
typedef unsigned short u16;
typedef u32 v4u __attribute__((ext_vector_type(4)));
typedef u32 v3u __attribute__((ext_vector_type(3)));
typedef u32 v2u __attribute__((ext_vector_type(2)));
typedef float v4f __attribute__((ext_vector_type(4)));
typedef float v2f __attribute__((ext_vector_type(2)));
typedef short v8s __attribute__((ext_vector_type(8)));
typedef short v4s __attribute__((ext_vector_type(4)));
typedef __bf16 v2b __attribute__((ext_vector_type(2)));
#define LAS __attribute__((address_space(3)))
#define GAS __attribute__((address_space(1)))
#define CAS __attribute__((address_space(4)))
#define DI __device__ __forceinline__

#define K_PREFETCH 1
#define K_EARLY_CK 3
#define K_SLEEP_1A 20
#define K_SLEEP_S3 0
#define K_SLEEP_H 8
#define K_SLEEP_O 6
#define K_SLEEP_UK 0
#define K_SLEEP_CK 0
#define K_CK_BAR 1
#define K_W1_EARLY 0
#define K_SLEEP_F 10

#define K_WS_END 1
#define WS_BASE(sz) (K_WS_END ? (((sz) - (16u << 20)) & ~(size_t)4095) : (size_t)0)
constexpr u32 WS_E = 0;
constexpr u32 WS_QP = 4096;
constexpr u32 WS_QG = WS_QP + 32 * 8 * 1024;
constexpr u32 WS_ST = WS_QG + 2 * 32 * 2048;
constexpr u32 WS_SL = WS_ST + 128 * 128;
constexpr u32 WS_HG = WS_SL + 128 * 128;
constexpr u32 WS_FG = WS_HG + 128 * 2 * 2304;
constexpr u32 WS_OG = WS_FG + 72 * 256 * 64;
constexpr u32 WS_END = WS_OG + 32 * 4 * 64 * 512;
constexpr u32 WS_DBG = 8u << 20;
constexpr u32 L1_Q = 131072, L1_MX = L1_Q + 16384, L1_P = L1_MX + 512, L1_XB = L1_P + 4352, L1_HB = L1_XB + 256;
constexpr u32 L1_W1 = 0, L1_W2 = 46080;
constexpr u32 L2_UV = 114688, L2_UK = 81920;
constexpr u32 L2_BQ = 147456, L2_BC = L2_BQ + 4352, L2_CT = L2_BC + 4352, L2_F = L2_CT + 544, L2_IL = L2_F + 256, L2_RD = L2_IL + 16, L2_X = L2_RD + 512, L2_GW = L2_X + 3072;
static_assert(L1_HB + 256 <= 163840 && L2_GW + 2048 <= 163840, "lds");
static_assert(L1_XB % 16 == 0 && L2_X % 16 == 0 && L2_GW % 16 == 0 && L2_RD % 16 == 0, "align");
constexpr int SC1 = 16, NT = 2;
constexpr u32 SPIN_CAP = 1u << 15;

#define SLEEP(idx, dflt) do { if (dflt) __builtin_amdgcn_s_sleep(dflt); } while (0)
#define KNOB(idx, dflt) (dflt)
#define STAMP(i) do {} while (0)
#define COUNT(i) do {} while (0)
#define STAMP_OUT() do {} while (0)

DI v4u mk_srd(u64 a, u32 bytes) {
  return v4u{(u32)a, (u32)(a >> 32) & 0xffffu, bytes, 0x00020000u};
}
DI __amdgpu_buffer_rsrc_t mk_rsrc(u64 p, u32 bytes) { return __builtin_amdgcn_make_buffer_rsrc((void*)p, 0, (int)bytes, 0x00020000); }
DI void dma(v4u srd, u32 voff, u32 soff, u32 lds) {
  asm volatile("s_mov_b32 m0, %3\n\ts_nop 0\n\tbuffer_load_dwordx4 %0, %1, %2 offen nt lds" ::"v"(voff), "s"(srd), "s"(soff), "s"(lds) : "memory");
}
#define BAR() asm volatile("s_waitcnt lgkmcnt(0)\n\ts_barrier" ::: "memory")
#define RAWBAR() asm volatile("s_barrier" ::: "memory")
#define VMW(n) asm volatile("s_waitcnt vmcnt(" #n ")" ::: "memory")
#define ORDER() asm volatile("" ::: "memory")
#define PIN(x) asm volatile("" : "+v"(x))
#define SB() __builtin_amdgcn_sched_barrier(0)
DI v4f mfma(v4u a, v4u b, v4f c) { return __builtin_amdgcn_mfma_f32_16x16x32_bf16(__builtin_bit_cast(v8s, a), __builtin_bit_cast(v8s, b), c, 0, 0, 0); }
DI u32 pk(float a, float b) { return __builtin_bit_cast(u32, __builtin_convertvector(v2f{a, b}, v2b)); }
DI float bflo(u32 x) { return __builtin_bit_cast(float, x << 16); }
DI float bfhi(u32 x) { return __builtin_bit_cast(float, x & 0xffff0000u); }
DI float f_(u32 x) { return __builtin_bit_cast(float, x); }
DI u32 u_(float x) { return __builtin_bit_cast(u32, x); }
template <int C> DI float dpp(float x) { return f_((u32)__builtin_amdgcn_update_dpp(0, (int)u_(x), C, 0xf, 0xf, true)); }
DI float sum8(float x) { x += dpp<0xB1>(x); x += dpp<0x4E>(x); x += dpp<0x141>(x); return x; }
DI float sum16(float x) { x = sum8(x); x += dpp<0x140>(x); return x; }
DI float add16(float x) { auto r = __builtin_amdgcn_permlane16_swap(u_(x), u_(x), false, false); return f_(r[0]) + f_(r[1]); }
DI float add32(float x) { auto r = __builtin_amdgcn_permlane32_swap(u_(x), u_(x), false, false); return f_(r[0]) + f_(r[1]); }
DI float max16(float x) { auto r = __builtin_amdgcn_permlane16_swap(u_(x), u_(x), false, false); return fmaxf(f_(r[0]), f_(r[1])); }
DI float max32(float x) { auto r = __builtin_amdgcn_permlane32_swap(u_(x), u_(x), false, false); return fmaxf(f_(r[0]), f_(r[1])); }
DI v4u lds128(LAS char* L, u32 a) { return *(LAS v4u*)(L + a); }
DI v2u ldstr(LAS char* L, u32 a) { return __builtin_bit_cast(v2u, __builtin_amdgcn_ds_read_tr16_b64_v4i16((LAS v4s*)(L + a))); }
DI v4u cat(v2u a, v2u b) { return v4u{a.x, a.y, b.x, b.y}; }
DI bool tagok(v4u a, u32 tag) { return (a.y == tag) & (a.w == tag); }
DI v4u tagged(v4f a, u32 tag) { return v4u{pk(a[0], a[1]), tag, pk(a[2], a[3]), tag}; }
DI u32 sel4(v4f a0, v4f a1, v4f a2, v4f a3, u32 g) {
  u32 p0 = pk(a0[0], a0[1]), p1 = pk(a1[0], a1[1]), p2 = pk(a2[0], a2[1]), p3 = pk(a3[0], a3[1]);
  p0 = g & 1 ? p1 : p0;
  p2 = g & 1 ? p3 : p2;
  return g & 2 ? p2 : p0;
}
DI v4u ld16(__amdgpu_buffer_rsrc_t r, u32 o) { return __builtin_amdgcn_raw_buffer_load_b128(r, o, 0, SC1); }
DI v2u ld8(__amdgpu_buffer_rsrc_t r, u32 o) { return __builtin_amdgcn_raw_buffer_load_b64(r, o, 0, SC1); }

typedef u32 v16u __attribute__((ext_vector_type(16)));
typedef u32 v8u __attribute__((ext_vector_type(8)));
DI u64 ptr(u32 lo, u32 hi) { return (u64)lo | ((u64)hi << 32); }

__global__ void __launch_bounds__(512, 2) fused_decode(const u16* x_, const u16* ckv_, const u16* wq_, const u16* wuk_, const u16* wuv_, const u16* wo_,
    const u16* w1_, const u16* b1_, const u16* w2_, const u16* b2_, const u16* gw_, u16* out_, char* ws_
    ) {
  __shared__ __attribute__((aligned(1024))) char smem[163840];
  LAS char* L = (LAS char*)smem;
  STAMP(0);
  const u32 tid = threadIdx.x, lane = tid & 63, w = __builtin_amdgcn_readfirstlane(tid >> 6);
  const u32 blk = blockIdx.x, xc = blk & 7, ii = blk >> 3;
  v16u ka; v8u kb; v2u kc;
  u64 pc0; u32 t0_, t1_;
  asm volatile(
      "s_load_dwordx16 %0, %6, 0x0\n\ts_load_dwordx8 %1, %6, 0x40\n\ts_load_dwordx2 %2, %6, 0x60\n\t"
      "s_getpc_b64 %3\n.Lpf_begin:\n\t"
      "s_min_u32 %4, %7, .Lpf_end-.Lpf_begin\n\ts_load_dword %4, %3, %4\n\t"
      "s_waitcnt lgkmcnt(0)"
      : "=&s"(ka), "=&s"(kb), "=&s"(kc), "=&s"(pc0), "=&s"(t0_), "=&s"(t1_)
      : "s"(__builtin_amdgcn_kernarg_segment_ptr()), "s"((ii * 8 + w) * 64)
      : "memory", "scc");
  const GAS u16* x = (const GAS u16*)ptr(ka[0], ka[1]);
  const u64 ckv = ptr(ka[2], ka[3]), wq = ptr(ka[4], ka[5]), wuk = ptr(ka[6], ka[7]), wuv = ptr(ka[8], ka[9]), wo = ptr(ka[10], ka[11]), w1 = ptr(ka[12], ka[13]);
  const GAS u16* b1 = (const GAS u16*)ptr(ka[14], ka[15]);
  const u64 w2 = ptr(kb[0], kb[1]), gw = ptr(kb[4], kb[5]);
  const GAS u16* b2 = (const GAS u16*)ptr(kb[2], kb[3]);
  GAS u16* out = (GAS u16*)ptr(kb[6], kb[7]);
  const u64 ws = ptr(kc[0], kc[1]);
  const u32 lr = lane & 15, lg = lane >> 4;
  const __amdgpu_buffer_rsrc_t rws = mk_rsrc(ws, WS_END);
  const u32 epoch = *(const CAS u32*)(ws + WS_E);
  auto get_tag = [&]() { u32 t = epoch; asm volatile("s_add_u32 %0, %0, 1" : "+s"(t)::"scc"); return t; };
  const bool g2role = ii < 16;
  const u32 k = ii & 15;
  const u32 ha = 4 * xc + (ii >> 3), jm = ii & 7;
  const u32 h = 4 * xc + (k >> 2), j = k & 3;
  const u32 g1 = 16 * xc + k, hg = g1 & 1, tile = g1 >> 1, b = tile >> 5;
  const v4u sck = mk_srd(ckv, 2 * 4096 * 1024);
  auto ck_dma = [&](u32 i0, u32 i1) {
#pragma nounroll
    for (u32 i = i0; i < i1; ++i) {
      const u32 row = 16 * w + (i ^ (8 * hg));
      dma(sck, (lane * 16) ^ ((((row & 3) << 2) | ((row >> 2) & 3)) << 4), (tile * 128 + row) * 1024, row * 1024);
    }
  };
  if (w == 0) {
    v4u wqv[18], xa[18];
    v3u xs[3];
    const __amdgpu_buffer_rsrc_t rx = mk_rsrc((u64)x, 2304), rq = mk_rsrc(wq, KNOB(8, 0) & 4 ? 0 : 576 * 8192);
    const u32 xo = ((lr >> 3) * 576 + 72 * jm + 18 * lg) * 2;
#pragma unroll
    for (int i = 0; i < 3; ++i) xs[i] = __builtin_amdgcn_raw_buffer_load_b96(rx, xo + 12 * i, 0, 0);
    const u32 vo = (72 * jm + 18 * lg) * 8192 + ha * 256 + lr * 16;
#pragma unroll
    for (int s = 0; s < 18; ++s) { wqv[s] = __builtin_amdgcn_raw_buffer_load_b128(rq, vo, s * 8192, NT); SB(); }
    RAWBAR();
    const u32 di = (lr & 7) >> 1, sh = (lr & 1) * 16;
    u32 se[4], so[4];
#pragma unroll
    for (int i = 0; i < 4; ++i) {
      se[i] = di == (u32)i ? ((0x0100u << sh) | (0x0c0c0000u >> sh)) : 0x0c0c0c0cu;
      so[i] = di == (u32)i ? ((0x0302u << sh) | (0x0c0c0000u >> sh)) : 0x0c0c0c0cu;
    }
#pragma unroll
    for (int s = 0; s < 18; ++s)
#pragma unroll
      for (int i = 0; i < 4; ++i) xa[s][i] = __builtin_amdgcn_perm(0u, xs[s / 6][(s % 6) >> 1], (s & 1) ? so[i] : se[i]);
    u32 qo = WS_QP + (ha * 8 + jm) * 1024 + lane * 16;
    PIN(qo);
    const u32 tag = get_tag();
    SB();
    v4f acc = {0.f, 0.f, 0.f, 0.f};
#pragma unroll
    for (int s = 0; s < 18; ++s) acc = mfma(xa[s], wqv[s], acc);
    __builtin_amdgcn_raw_buffer_store_b128(tagged(acc, tag), rws, qo, 0, SC1);
  } else {
    RAWBAR();
    if (g2role) {
      SLEEP(0, K_SLEEP_UK);
      const v4u suk = mk_srd(wuk, 512 * 8192);
#pragma nounroll
      for (u32 n = w == 1 ? 0 : 4 * w; n < 4 * w + (KNOB(8, 0) & 8 ? 0 : 4); ++n) {
        const u32 row = 4 * (n & 3) + lg;
        dma(suk, row * 8192 + ((lr ^ row) << 4), (128 * j + 4 * (n & ~3u)) * 8192 + h * 256, L2_UK + n * 1024);
      }
      if (w == 7) {
        const v4u sx = mk_srd((u64)x, 2304), sg = mk_srd(gw, 1152);
        dma(sx, lane * 16, 0, L2_X); dma(sx, lane * 16, 1024, L2_X + 1024); dma(sx, lane * 16, 2048, L2_X + 2048);
        dma(sg, lane * 16, 0, L2_GW); dma(sg, lane * 16, 1024, L2_GW + 1024);
      }
    } else {
      ck_dma(0, KNOB(2, K_EARLY_CK));
      SLEEP(1, K_SLEEP_CK);
    }
  }
  STAMP(1);
  const u32 q4 = (lane >> 2) & 3, p4 = lane & 3;
  const u32 fr0 = 4 * (lg & 1) + q4, fr1 = fr0 ^ 4, fpc = 2 * (w & 3) + (p4 >> 1);
  const u32 fa0 = fr0 * 128 + ((fpc ^ (fr0 & 6)) << 4) + 8 * (p4 & 1), fa1 = fr1 * 128 + ((fpc ^ (fr1 & 6)) << 4) + 8 * (p4 & 1);
  const u32 rr = lane >> 3, pcs = ((lane & 7) ^ (rr & 6)) << 4;
  auto frag = [&](u32 a) { return cat(ldstr(L, a + fa0), ldstr(L, a + fa1)); };

  if (g2role) {
    STAMP(14);
    const bool reducer = k < 9;
    const u32 rho = 9 * xc + k, rmt = rho >> 1, rb_ = rho & 1;
    u32 b2v = 0;
    if (reducer && tid < 16 && !(KNOB(8, 0) & 256)) b2v = b2[8 * rho + (tid >> 1)];
    u32 aqp = WS_QP + (h * 8 + w) * 1024 + lane * 16, abq = L2_BQ + ((lg >> 1) * 8 + w) * 272 + (8 * lr + 4 * (lg & 1)) * 2;
    u32 auk = L2_UK + w * 4096 + lr * 256, abr = lr * 272 + 16 * lg, aqg = WS_QG + ((lg >> 1) * 32 + h) * 2048 + (32 * j + 4 * w + (lr >> 2)) * 16;
    u32 auk0 = auk + ((lg ^ lr) << 4), auk1 = auk + (((4 + lg) ^ lr) << 4), auk2 = auk + (((8 + lg) ^ lr) << 4), auk3 = auk + (((12 + lg) ^ lr) << 4);
    PIN(aqp); PIN(abq); PIN(abr); PIN(aqg); PIN(auk0); PIN(auk1); PIN(auk2); PIN(auk3);
    STAMP(15);
    RAWBAR();
    STAMP(13);
    const u32 tag = get_tag();
    SLEEP(3, K_SLEEP_1A);
    v4u qp;
#pragma nounroll
    for (u32 sp = 0; sp < SPIN_CAP; ++sp) {
      ORDER();
      qp = ld16(rws, aqp);
      COUNT(0);
      if (__all(tagok(qp, tag))) break;
    }
    *(LAS v2u*)(L + abq) = v2u{qp.x, qp.z};
    PIN(b2v);
    VMW(0);
    STAMP(2);
    BAR();
    {
      const v4u a0 = lds128(L, auk0), b0 = lds128(L, L2_BQ + abr), a1 = lds128(L, auk1), b1_ = lds128(L, L2_BQ + abr + 64);
      const v4u a2 = lds128(L, auk2), b2_ = lds128(L, L2_BQ + abr + 128), a3 = lds128(L, auk3), b3 = lds128(L, L2_BQ + abr + 192);
      SB();
      v4f acc = {0.f, 0.f, 0.f, 0.f};
      acc = mfma(b0, a0, acc); acc = mfma(b1_, a1, acc); acc = mfma(b2_, a2, acc); acc = mfma(b3, a3, acc);
      const float sc = 0.08838834764831845f * 1.4426950408889634f;
      const float v = add16((acc[0] + acc[1]) + (acc[2] + acc[3])) * sc;
      const u32 p01 = pk(v, dpp<0xB1>(v));
      const u32 p23 = __builtin_amdgcn_update_dpp(0, (int)p01, 0x4E, 0xf, 0xf, true);
      if ((lane & 19) == 0) __builtin_amdgcn_raw_buffer_store_b128(v4u{p01, tag, p23, tag}, rws, aqg, 0, SC1);
    }
    STAMP(3);
    BAR();
    SLEEP(4, K_SLEEP_S3);
    const v4u suv = mk_srd(wuv, 512 * 8192), swo = mk_srd(wo, 4096 * 1152);
#pragma nounroll
    for (u32 slot = w; slot < 32; slot += 8)
      dma(suv, (16 * (rr & 3) + (rr >> 2)) * 8192 + pcs, (128 * j + 64 * (slot >> 4) + (slot & 14)) * 8192 + h * 256 + (slot & 1) * 128, L2_UV + slot * 1024);
    auto wo_dma = [&](u32 s0, u32 s1) {
#pragma nounroll
      for (u32 slot = s0; slot < s1; slot += 8) {
        const u32 rbl = (slot * 57) >> 9, cg = slot - 9 * rbl;
        dma(swo, rr * 1152 + pcs, (h * 128 + 8 * ((rbl + 4 * j) & 15)) * 1152 + cg * 128, slot * 1024);
      }
    };
    wo_dma(w, 48);
    VMW(6);
    BAR();
    v4u uvA[4];
#pragma unroll
    for (int s = 0; s < 4; ++s) uvA[s] = frag(L2_UV + ((4 * s + lg) * 2 + (w >> 2)) * 1024);
    float gate0 = 0.f, gate1 = 0.f;
    if (reducer) {
      float a0 = 0.f, a1 = 0.f;
#pragma unroll
      for (int i = 0; i < 5; ++i) {
        const u32 e = lane + 64 * i;
        const u32 x0 = *(LAS u32*)(L + L2_X + e * 4), x1 = *(LAS u32*)(L + L2_X + 1152 + e * 4), gv = *(LAS u32*)(L + L2_GW + e * 4);
        a0 += e < 288 ? bflo(x0) * bflo(gv) + bfhi(x0) * bfhi(gv) : 0.f;
        a1 += e < 288 ? bflo(x1) * bflo(gv) + bfhi(x1) * bfhi(gv) : 0.f;
      }
      gate0 = 1.f / (1.f + __expf(-add32(add16(sum16(a0)))));
      gate1 = 1.f / (1.f + __expf(-add32(add16(sum16(a1)))));
    }
    BAR();
    wo_dma(48 + w, 144);
    STAMP(4);
    u32 sto = (lane * 2 + (h >> 4)) * 128 + (h & 15) * 8;
    u32 ogo = WS_OG + ((h * 4 + j) * 64 + 8 * w + lg) * 512 + lr * 16;
    u32 awo = lg * 9216 + (w >> 2) * 1024, af = L2_F + (8 * w + lg) * 4;
    const u32 U1 = 16 * (lg & 1) + lr, U2 = 4 * w + (lr >> 2);
    u32 abc = L2_BC + ((w >> 2) * 8 + (w & 3) * 2 + (lg >> 1)) * 272 + (U1 ^ ((U1 >> 1) & 1)) * 8;
    u32 act = L2_CT + (lg >> 1) * 272 + (U2 ^ ((U2 >> 1) & 1)) * 8 + (lr & 3) * 2, ail = L2_IL + (lg >> 1) * 4;
    u32 acr[4];
#pragma unroll
    for (int s = 0; s < 4; ++s) { acr[s] = L2_CT + (lr & 1) * 272 + (32 * ((s + j) & 3) + 8 * lg) * 2; PIN(acr[s]); }
    u32 fo = WS_FG + (128 + h * 4 + j) * 128 + lr * 8 + (w + 8 * lg) * 32768, fro = WS_FG + rmt * 32768 + (tid >> 2) * 128 + rb_ * 64 + (tid & 3) * 16;
    u32 sel[4];
    {
      const u32 di = (lr & 7) >> 1, sh = (lr & 1) * 16, src = (lr >> 3) ? 0x0302u : 0x0100u;
#pragma unroll
      for (int i = 0; i < 4; ++i) { sel[i] = di == (u32)i ? ((src << sh) | (0x0c0c0000u >> sh)) : 0x0c0c0c0cu; PIN(sel[i]); }
    }
    PIN(sto); PIN(ogo); PIN(awo); PIN(af); PIN(abc); PIN(act); PIN(ail); PIN(fo); PIN(fro); PIN(gate0); PIN(gate1);
    VMW(0);
    STAMP(5);
    float fch = 0.f;
    if (w == 0) {
      v2u mv;
#pragma nounroll
      for (u32 sp = 0; sp < SPIN_CAP; ++sp) {
        ORDER();
        mv = ld8(rws, WS_ST + sto);
        COUNT(1);
        if (__all(mv.y == tag)) break;
      }
      float M = f_(mv.x);
      M = fmaxf(M, dpp<0xB1>(M)); M = fmaxf(M, dpp<0x4E>(M)); M = fmaxf(M, dpp<0x141>(M)); M = fmaxf(M, dpp<0x140>(M)); M = max16(M);
      fch = __builtin_amdgcn_exp2f(f_(mv.x) - M);
      const u32 hi = pk(fch, 0.f) & 0xffffu;
      *(LAS u32*)(L + L2_F + lane * 4) = pk(fch, fch - bflo(hi));
    }
    BAR();
    SLEEP(5, K_SLEEP_O);
    STAMP(6);
    v4u og[4];
    v2u lv = {0u, tag};
#pragma unroll
    for (int e = 0; e < 4; ++e) og[e] = ld16(rws, ogo + (e >> 1) * 2048 + (e & 1) * 256);
    if (w == 0) lv = ld8(rws, WS_SL + sto);
    ORDER();
    v4u woA[5][4], fA[2];
#pragma unroll
    for (int e = 0; e < 2; ++e) {
      const u32 fv = *(LAS u32*)(L + af + 16 * e);
#pragma unroll
      for (int i = 0; i < 4; ++i) fA[e][i] = __builtin_amdgcn_perm(0u, fv, sel[i]);
    }
#pragma unroll
    for (int n = 0; n < 4; ++n)
#pragma unroll
      for (int s = 0; s < 4; ++s) woA[n][s] = frag(awo + s * 36864 + n * 2048);
#pragma unroll
    for (int s = 0; s < 4; ++s) woA[4][s] = frag(lg * 9216 + s * 36864 + 8192);
    ORDER();
#pragma nounroll
    for (u32 sp = 0; sp < SPIN_CAP; ++sp) {
      const bool k0 = __all(tagok(og[0], tag) & tagok(og[1], tag)), k1 = __all(tagok(og[2], tag) & tagok(og[3], tag) & (lv.y == tag));
      if (k0 & k1) break;
      COUNT(2);
      ORDER();
      if (!k0) { og[0] = ld16(rws, ogo); og[1] = ld16(rws, ogo + 256); }
      if (!k1) { og[2] = ld16(rws, ogo + 2048); og[3] = ld16(rws, ogo + 2304); if (w == 0) lv = ld8(rws, WS_SL + sto); }
    }
    STAMP(7);
    {
      v4f acc = {0.f, 0.f, 0.f, 0.f};
      acc = mfma(fA[0], v4u{og[0].x, og[0].z, og[1].x, og[1].z}, acc);
      acc = mfma(fA[1], v4u{og[2].x, og[2].z, og[3].x, og[3].z}, acc);
      *(LAS v2u*)(L + abc) = v2u{pk(acc[0], acc[1]), pk(acc[2], acc[3])};
      if (w == 0) {
        const float ls = add16(sum16(fch * f_(lv.x)));
        if ((lane & 31) == 0) *(LAS float*)(L + L2_IL + (lane >> 5) * 4) = 1.f / ls;
      }
    }
    BAR();
    {
      const v4u c0 = lds128(L, L2_BC + abr), c1 = lds128(L, L2_BC + abr + 64), c2 = lds128(L, L2_BC + abr + 128), c3 = lds128(L, L2_BC + abr + 192);
      const float il = *(LAS float*)(L + ail);
      SB();
      v4f acc = {0.f, 0.f, 0.f, 0.f};
      acc = mfma(c0, uvA[0], acc); acc = mfma(c1, uvA[1], acc); acc = mfma(c2, uvA[2], acc); acc = mfma(c3, uvA[3], acc);
      const float v = add16((acc[0] + acc[1]) + (acc[2] + acc[3])) * il;
      if (!(lg & 1)) *(LAS u16*)(L + act) = (u16)pk(v, v);
    }
    BAR();
    STAMP(8);
    {
      v4u cb[4];
#pragma unroll
      for (int s = 0; s < 4; ++s) cb[s] = lds128(L, acr[s]);
      SB();
      v4f acc[5];
#pragma unroll
      for (int n = 0; n < 5; ++n) acc[n] = v4f{0.f, 0.f, 0.f, 0.f};
#pragma unroll
      for (int n = 0; n < 4; ++n)
#pragma unroll
        for (int s = 0; s < 4; ++s) acc[n] = mfma(cb[s], woA[n][s], acc[n]);
      __builtin_amdgcn_raw_buffer_store_b64(v2u{sel4(acc[0], acc[1], acc[2], acc[3], lg), tag}, rws, fo, 0, SC1);
      if (w < 4) {
#pragma unroll
        for (int s = 0; s < 4; ++s) acc[4] = mfma(cb[s], woA[4][s], acc[4]);
        if (lane < 16) __builtin_amdgcn_raw_buffer_store_b64(v2u{pk(acc[4][0], acc[4][1]), tag}, rws, fo, 32 * 32768, SC1);
      }
    }
    STAMP(9);
    if (reducer) {
      SLEEP(7, K_SLEEP_F);
      v4u pm = ld16(rws, fro), pa = ld16(rws, fro + 16384);
#pragma nounroll
      for (u32 sp = 0; sp < SPIN_CAP; ++sp) {
        const bool k0 = __all(tagok(pm, tag)), k1 = __all(tagok(pa, tag));
        if (k0 & k1) break;
        COUNT(3);
        ORDER();
        if (!k0) pm = ld16(rws, fro);
        if (!k1) pa = ld16(rws, fro + 16384);
      }
      STAMP(10);
      v4f v = {gate0 * bflo(pa.x) + (1.f - gate0) * bflo(pm.x), gate1 * bfhi(pa.x) + (1.f - gate1) * bfhi(pm.x),
               gate0 * bflo(pa.z) + (1.f - gate0) * bflo(pm.z), gate1 * bfhi(pa.z) + (1.f - gate1) * bfhi(pm.z)};
#pragma unroll
      for (int i = 0; i < 4; ++i) {
        float t = v[i];
        t += dpp<0x124>(t); t += dpp<0x128>(t);
        v[i] = add32(add16(t));
      }
      if (lane < 4) *(LAS v4f*)(L + L2_RD + w * 64 + lane * 16) = v;
      BAR();
      if (tid < 16) {
        float s = (1.f - (tid & 1 ? gate1 : gate0)) * bflo(b2v);
#pragma unroll
        for (int i = 0; i < 8; ++i) s += *(LAS float*)(L + L2_RD + i * 64 + tid * 4);
        out[(tid & 1) * 576 + 8 * rho + (tid >> 1)] = (u16)pk(s, 0.f);
      }
      if (blk == 0 && tid == 0) __hip_atomic_store((GAS u32*)(ws + WS_E), tag, __ATOMIC_RELAXED, __HIP_MEMORY_SCOPE_AGENT);
      STAMP(11);
    }
  } else {
    const u32 hu = tid >> 4, hf = 2 * hu + ((lr >> 1) & 1);
    u32 b1v = 0, xv = 0;
    if (tid < 288 && lr < 4 && !(KNOB(8, 0) & 512)) b1v = b1[36 * g1 + hf];
    if (tid < 128 && lane < 36 && !(KNOB(8, 0) & 512)) xv = x[w * 576 + 36 * k + lane];
    if (KNOB(9, K_CK_BAR)) RAWBAR();
    ck_dma(w == 0 ? 0 : KNOB(2, K_EARLY_CK), 16);
    STAMP(2);
    const u32 sg = (0x9cu >> (2 * lg)) & 3;
    const u32 pir = ((lr & 3) << 2) | (lr >> 2);
    u32 qo = WS_QG + (b * 32 + hg * 16) * 2048 + tid * 16;
    u32 akr[4], aqr[4], aqw[4];
#pragma unroll
    for (int s = 0; s < 4; ++s) {
      akr[s] = (16 * w + lr) * 1024 + (((4 * s + sg) ^ pir) << 4);
      aqr[s] = L1_Q + lr * 1024 + (((4 * s + sg) ^ lr) << 4);
      const u32 head = 4 * s + (w >> 1), c4 = tid & 127;
      aqw[s] = L1_Q + head * 1024 + (((c4 >> 1) ^ head) << 4) + (c4 & 1) * 8;
      PIN(akr[s]); PIN(aqr[s]); PIN(aqw[s]);
    }
    u32 amx = L1_MX + lr * 32, apw = L1_P + lr * 272 + (16 * w + 4 * lg) * 2, apr = L1_P + lr * 272 + 16 * lg;
    u32 oo = WS_OG + (((hg * 16 + 4 * lg) * 4 + (w >> 1)) * 64 + tile) * 512 + (w & 1) * 256 + lr * 16, sto = g1 * 128 + lr * 8;
    PIN(qo); PIN(amx); PIN(apw); PIN(apr); PIN(oo); PIN(sto);
    VMW(0);
    const u32 tag = get_tag();
    PIN(b1v); PIN(xv);
    STAMP(3);
    v4u qg[4];
#pragma unroll
    for (int u = 0; u < 4; ++u) qg[u] = ld16(rws, qo + u * 8192);
    ORDER();
    v4u kA[16];
#pragma unroll
    for (int s = 0; s < 16; ++s) kA[s] = lds128(L, akr[s & 3] + (s >> 2) * 256);
    if (tid < 128) {
      const u32 U = lane >> 2;
      *(LAS u16*)(L + L1_XB + w * 128 + (U ^ ((U >> 1) & 1)) * 8 + (lane & 3) * 2) = (u16)xv;
      *(LAS u16*)(L + L1_HB + tid * 2) = 0;
    }
    BAR();
    v4u vA[4][4];
#pragma unroll
    for (int tt = 0; tt < 4; ++tt) {
      const u32 a0 = (8 * lg + q4) * 1024 + (((8 * w + 2 * tt + (p4 >> 1)) ^ ((q4 << 2) | (2 * (lg & 1)))) << 4) + 8 * (p4 & 1);
      const u32 a1 = (a0 ^ 16) + 4096;
#pragma unroll
      for (int t = 0; t < 4; ++t) vA[tt][t] = cat(ldstr(L, a0 + t * 32768), ldstr(L, a1 + t * 32768));
    }
    ORDER();
#pragma nounroll
    for (u32 sp = 0; sp < SPIN_CAP; ++sp) {
      const bool k0 = __all(tagok(qg[0], tag)), k1 = __all(tagok(qg[1], tag)), k2 = __all(tagok(qg[2], tag)), k3 = __all(tagok(qg[3], tag));
      if (k0 & k1 & k2 & k3) break;
      COUNT(0);
      ORDER();
      if (!k0) qg[0] = ld16(rws, qo);
      if (!k1) qg[1] = ld16(rws, qo + 8192);
      if (!k2) qg[2] = ld16(rws, qo + 16384);
      if (!k3) qg[3] = ld16(rws, qo + 24576);
    }
#pragma unroll
    for (int u = 0; u < 4; ++u) *(LAS v2u*)(L + aqw[u]) = v2u{qg[u].x, qg[u].z};
    STAMP(4);
    BAR();
    const v4u sw1 = mk_srd(w1, 576 * 9216), sw2 = mk_srd(w2, 4608 * 1152);
    auto w_dma1 = [&](v4u srd, u32 pitch, u32 base, u32 lds, u32 slot) {
      const u32 rbl = (slot * 57) >> 9, cg = slot - 9 * rbl;
      dma(srd, (slot >= 36 && rr >= 4 ? 0x80000000u : rr * pitch) + pcs, base + 8 * rbl * pitch + cg * 128, lds + slot * 1024);
    };
    auto w_dma = [&](v4u srd, u32 pitch, u32 base, u32 lds) {
#pragma nounroll
      for (u32 slot = w; slot < 45; slot += 8) w_dma1(srd, pitch, base, lds, slot);
    };
    v4f S = {0.f, 0.f, 0.f, 0.f};
    {
      v4u qB[16];
#pragma unroll
      for (int s = 0; s < 16; ++s) qB[s] = lds128(L, aqr[s & 3] + (s >> 2) * 256);
      SB();
#pragma unroll
      for (int s = 0; s < 16; ++s) {
        S = mfma(kA[s], qB[s], S);
      }
    }
    {
      const float mw = max32(max16(fmaxf(fmaxf(S[0], S[1]), fmaxf(S[2], S[3]))));
      if (lane < 16) *(LAS float*)(L + amx + w * 4) = mw;
    }
    BAR();
    STAMP(5);
    {
      const v4f m0 = *(LAS v4f*)(L + amx), m1 = *(LAS v4f*)(L + amx + 16);
      const float M = fmaxf(fmaxf(fmaxf(m0[0], m0[1]), fmaxf(m0[2], m0[3])), fmaxf(fmaxf(m1[0], m1[1]), fmaxf(m1[2], m1[3])));
      const float p0 = __builtin_amdgcn_exp2f(S[0] - M), p1 = __builtin_amdgcn_exp2f(S[1] - M), p2 = __builtin_amdgcn_exp2f(S[2] - M), p3 = __builtin_amdgcn_exp2f(S[3] - M);
      *(LAS v2u*)(L + apw) = v2u{pk(p0, p1), pk(p2, p3)};
      if (tid < 16) __builtin_amdgcn_raw_buffer_store_b64(v2u{u_(M), tag}, rws, sto, WS_ST, SC1);
    }
    BAR();
    {
      v4u pB[4];
#pragma unroll
      for (int t = 0; t < 4; ++t) pB[t] = lds128(L, apr + 64 * t);
      SB();
      v4f o[4];
#pragma unroll
      for (int tt = 0; tt < 4; ++tt) o[tt] = v4f{0.f, 0.f, 0.f, 0.f};
#pragma unroll
      for (int t = 0; t < 4; ++t)
#pragma unroll
        for (int tt = 0; tt < 4; ++tt) o[tt] = mfma(pB[t], vA[tt][t], o[tt]);
#pragma unroll
      for (int r = 0; r < 4; ++r)
        __builtin_amdgcn_raw_buffer_store_b128(v4u{pk(o[0][r], o[1][r]), tag, pk(o[2][r], o[3][r]), tag}, rws, oo, r * 4 * 64 * 512, SC1);
      if (w == 7) {
        v4f acc = {0.f, 0.f, 0.f, 0.f};
        const v4u ones = {0x3f803f80u, 0x3f803f80u, 0x3f803f80u, 0x3f803f80u};
#pragma unroll
        for (int t = 0; t < 4; ++t) acc = mfma(ones, pB[t], acc);
        if (lane < 16) __builtin_amdgcn_raw_buffer_store_b64(v2u{u_(acc[0]), tag}, rws, sto, WS_SL, SC1);
      }
    }
    STAMP(6);
    w_dma(sw1, 9216, 36 * k * 9216 + xc * 1152, L1_W1);
    w_dma(sw2, 1152, 36 * g1 * 1152, L1_W2);
    auto gemv36 = [&](u32 lw, u32 lb, u32 vo, u32 stride) {
      const v4u h0 = lds128(L, lb + (lr & 1) * 128 + 16 * lg), h1 = lds128(L, lb + (lr & 1) * 128 + 64 + 16 * lg);
      v4u a0[5], a1[5];
#pragma unroll
      for (int n = 0; n < 5; ++n) {
        const u32 cg = n < 4 ? 2 * n + (w >> 2) : 8;
        a0[n] = frag(lw + (lg * 9 + cg) * 1024);
        a1[n] = frag(lw + (36 + cg) * 1024);
      }
      SB();
      v4f acc[5];
#pragma unroll
      for (int n = 0; n < 5; ++n) acc[n] = mfma(h1, a1[n], mfma(h0, a0[n], v4f{0.f, 0.f, 0.f, 0.f}));
      __builtin_amdgcn_raw_buffer_store_b64(v2u{sel4(acc[0], acc[1], acc[2], acc[3], lg), tag}, rws, vo, 0, SC1);
      if (w < 4 && lane < 16) __builtin_amdgcn_raw_buffer_store_b64(v2u{pk(acc[4][0], acc[4][1]), tag}, rws, vo, 32 * stride, SC1);
    };
    if (w < 5) { VMW(6); } else { VMW(5); }
    BAR();
    STAMP(7);
    gemv36(L1_W1, L1_XB, WS_HG + g1 * 4608 + (w + 8 * lg) * 128 + lr * 8, 128);
    STAMP(8);
    SLEEP(6, K_SLEEP_H);
    if (w < 5) {
      const u32 ho = WS_HG + (16 * xc + lr) * 4608 + (18 * k + min(hu, 17u)) * 16;
      v4u hp;
#pragma nounroll
      for (u32 sp = 0; sp < SPIN_CAP; ++sp) {
        ORDER();
        hp = ld16(rws, ho);
        COUNT(1);
        if (__all(tagok(hp, tag))) break;
      }
      const float s0 = sum16(bflo(hp.x)), s1 = sum16(bfhi(hp.x)), s2 = sum16(bflo(hp.z)), s3 = sum16(bfhi(hp.z));
      const float v = (lr == 0 ? s0 : lr == 1 ? s1 : lr == 2 ? s2 : s3) + bflo(b1v);
      const float u2 = 1.5957691216057308f * (v + 0.044715f * v * v * v);
      const float hv = v / (1.f + __expf(-u2));
      if (tid < 288 && lr < 4) *(LAS u16*)(L + L1_HB + (lr & 1) * 128 + ((hf >> 2) ^ ((hf >> 3) & 1)) * 8 + (hf & 3) * 2) = (u16)pk(hv, 0.f);
    }
    VMW(0);
    STAMP(9);
    BAR();
    gemv36(L1_W2, L1_HB, WS_FG + g1 * 128 + lr * 8 + (w + 8 * lg) * 32768, 32768);
    STAMP(10);
  }
  STAMP_OUT();
  asm volatile(".Lpf_end:");
}

extern "C" void kernel_launch(void* const* d_in, const int* in_sizes, int n_in, void* d_out, int out_size, void* d_ws, size_t ws_size, hipStream_t stream) {
  fused_decode<<<256, 512, 0, stream>>>((const u16*)d_in[0], (const u16*)d_in[1], (const u16*)d_in[2], (const u16*)d_in[3], (const u16*)d_in[4],
      (const u16*)d_in[5], (const u16*)d_in[7], (const u16*)d_in[8], (const u16*)d_in[9], (const u16*)d_in[10], (const u16*)d_in[11], (u16*)d_out, (char*)d_ws + WS_BASE(ws_size)
      );
}
```

```cpp
#define PROBE_ID 0
#define PROBE_BLK 0
#include <hip/hip_runtime.h>
#include <stdint.h>

typedef unsigned u32;
typedef unsigned long long u64;
typedef unsigned short u16;
typedef u32 v4u __attribute__((ext_vector_type(4)));
typedef u32 v3u __attribute__((ext_vector_type(3)));
typedef u32 v2u __attribute__((ext_vector_type(2)));
typedef float v4f __attribute__((ext_vector_type(4)));
typedef float v2f __attribute__((ext_vector_type(2)));
typedef short v8s __attribute__((ext_vector_type(8)));
typedef short v4s __attribute__((ext_vector_type(4)));
typedef __bf16 v2b __attribute__((ext_vector_type(2)));
#define LAS __attribute__((address_space(3)))
#define GAS __attribute__((address_space(1)))
#define CAS __attribute__((address_space(4)))
#define DI __device__ __forceinline__

#define K_PREFETCH 1
#define K_EARLY_CK 3
#define K_SLEEP_1A 20
#define K_SLEEP_S3 0
#define K_SLEEP_H 8
#define K_SLEEP_O 6
#define K_SLEEP_UK 0
#define K_SLEEP_CK 0
#define K_CK_BAR 1
#define K_W1_EARLY 0
#define K_SLEEP_F 10

#define K_WS_END 0
#define WS_BASE(sz) (K_WS_END ? (((sz) - (16u << 20)) & ~(size_t)4095) : (size_t)0)
constexpr u32 WS_E = 0;
constexpr u32 WS_QP = 4096;
constexpr u32 WS_QG = WS_QP + 32 * 8 * 1024;
constexpr u32 WS_ST = WS_QG + 2 * 32 * 2048;
constexpr u32 WS_SL = WS_ST + 128 * 128;
constexpr u32 WS_HG = WS_SL + 128 * 128;
constexpr u32 WS_FG = WS_HG + 128 * 2 * 2304;
constexpr u32 WS_OG = WS_FG + 72 * 256 * 64;
constexpr u32 WS_END = WS_OG + 32 * 4 * 64 * 512;
constexpr u32 WS_DBG = 8u << 20;
constexpr u32 L1_Q = 131072, L1_MX = L1_Q + 16384, L1_P = L1_MX + 512, L1_XB = L1_P + 4352, L1_HB = L1_XB + 256;
constexpr u32 L1_W1 = 0, L1_W2 = 46080;
constexpr u32 L2_UV = 114688, L2_UK = 81920;
constexpr u32 L2_BQ = 147456, L2_BC = L2_BQ + 4352, L2_CT = L2_BC + 4352, L2_F = L2_CT + 544, L2_IL = L2_F + 256, L2_RD = L2_IL + 16, L2_X = L2_RD + 512, L2_GW = L2_X + 3072;
static_assert(L1_HB + 256 <= 163840 && L2_GW + 2048 <= 163840, "lds");
static_assert(L1_XB % 16 == 0 && L2_X % 16 == 0 && L2_GW % 16 == 0 && L2_RD % 16 == 0, "align");
constexpr int SC1 = 16, NT = 2;
constexpr u32 SPIN_CAP = 1u << 15;

#define SLEEP(idx, dflt) do { if (dflt) __builtin_amdgcn_s_sleep(dflt); } while (0)
#define KNOB(idx, dflt) (dflt)
#define STAMP(i) do { if ((i) == 0) asm volatile("s_memrealtime %0\n\ts_waitcnt lgkmcnt(0)" : "=s"(pt0)::"memory"); if ((i) == PROBE_ID) asm volatile("s_memrealtime %0\n\ts_waitcnt lgkmcnt(0)" : "=s"(pt1)::"memory"); } while (0)
#define COUNT(i) do {} while (0)
#define STAMP_OUT() do { if (blk == PROBE_BLK && w == 0) { u64 t_; do { __builtin_amdgcn_s_sleep(2); asm volatile("s_memrealtime %0\n\ts_waitcnt lgkmcnt(0)" : "=s"(t_)::"memory"); } while (t_ - pt0 < 4000 + 4 * (pt1 - pt0)); } } while (0)

DI v4u mk_srd(u64 a, u32 bytes) {
  return v4u{(u32)a, (u32)(a >> 32) & 0xffffu, bytes, 0x00020000u};
}
DI __amdgpu_buffer_rsrc_t mk_rsrc(u64 p, u32 bytes) { return __builtin_amdgcn_make_buffer_rsrc((void*)p, 0, (int)bytes, 0x00020000); }
DI void dma(v4u srd, u32 voff, u32 soff, u32 lds) {
  asm volatile("s_mov_b32 m0, %3\n\ts_nop 0\n\tbuffer_load_dwordx4 %0, %1, %2 offen nt lds" ::"v"(voff), "s"(srd), "s"(soff), "s"(lds) : "memory");
}
#define BAR() asm volatile("s_waitcnt lgkmcnt(0)\n\ts_barrier" ::: "memory")
#define RAWBAR() asm volatile("s_barrier" ::: "memory")
#define VMW(n) asm volatile("s_waitcnt vmcnt(" #n ")" ::: "memory")
#define ORDER() asm volatile("" ::: "memory")
#define PIN(x) asm volatile("" : "+v"(x))
#define SB() __builtin_amdgcn_sched_barrier(0)
DI v4f mfma(v4u a, v4u b, v4f c) { return __builtin_amdgcn_mfma_f32_16x16x32_bf16(__builtin_bit_cast(v8s, a), __builtin_bit_cast(v8s, b), c, 0, 0, 0); }
DI u32 pk(float a, float b) { return __builtin_bit_cast(u32, __builtin_convertvector(v2f{a, b}, v2b)); }
DI float bflo(u32 x) { return __builtin_bit_cast(float, x << 16); }
DI float bfhi(u32 x) { return __builtin_bit_cast(float, x & 0xffff0000u); }
DI float f_(u32 x) { return __builtin_bit_cast(float, x); }
DI u32 u_(float x) { return __builtin_bit_cast(u32, x); }
template <int C> DI float dpp(float x) { return f_((u32)__builtin_amdgcn_update_dpp(0, (int)u_(x), C, 0xf, 0xf, true)); }
DI float sum8(float x) { x += dpp<0xB1>(x); x += dpp<0x4E>(x); x += dpp<0x141>(x); return x; }
DI float sum16(float x) { x = sum8(x); x += dpp<0x140>(x); return x; }
DI float add16(float x) { auto r = __builtin_amdgcn_permlane16_swap(u_(x), u_(x), false, false); return f_(r[0]) + f_(r[1]); }
DI float add32(float x) { auto r = __builtin_amdgcn_permlane32_swap(u_(x), u_(x), false, false); return f_(r[0]) + f_(r[1]); }
DI float max16(float x) { auto r = __builtin_amdgcn_permlane16_swap(u_(x), u_(x), false, false); return fmaxf(f_(r[0]), f_(r[1])); }
DI float max32(float x) { auto r = __builtin_amdgcn_permlane32_swap(u_(x), u_(x), false, false); return fmaxf(f_(r[0]), f_(r[1])); }
DI v4u lds128(LAS char* L, u32 a) { return *(LAS v4u*)(L + a); }
DI v2u ldstr(LAS char* L, u32 a) { return __builtin_bit_cast(v2u, __builtin_amdgcn_ds_read_tr16_b64_v4i16((LAS v4s*)(L + a))); }
DI v4u cat(v2u a, v2u b) { return v4u{a.x, a.y, b.x, b.y}; }
DI bool tagok(v4u a, u32 tag) { return (a.y == tag) & (a.w == tag); }
DI v4u tagged(v4f a, u32 tag) { return v4u{pk(a[0], a[1]), tag, pk(a[2], a[3]), tag}; }
DI u32 sel4(v4f a0, v4f a1, v4f a2, v4f a3, u32 g) {
  u32 p0 = pk(a0[0], a0[1]), p1 = pk(a1[0], a1[1]), p2 = pk(a2[0], a2[1]), p3 = pk(a3[0], a3[1]);
  p0 = g & 1 ? p1 : p0;
  p2 = g & 1 ? p3 : p2;
  return g & 2 ? p2 : p0;
}
DI v4u ld16(__amdgpu_buffer_rsrc_t r, u32 o) { return __builtin_amdgcn_raw_buffer_load_b128(r, o, 0, SC1); }
DI v2u ld8(__amdgpu_buffer_rsrc_t r, u32 o) { return __builtin_amdgcn_raw_buffer_load_b64(r, o, 0, SC1); }

typedef u32 v16u __attribute__((ext_vector_type(16)));
typedef u32 v8u __attribute__((ext_vector_type(8)));
DI u64 ptr(u32 lo, u32 hi) { return (u64)lo | ((u64)hi << 32); }

__global__ void __launch_bounds__(512, 2) fused_decode(const u16* x_, const u16* ckv_, const u16* wq_, const u16* wuk_, const u16* wuv_, const u16* wo_,
    const u16* w1_, const u16* b1_, const u16* w2_, const u16* b2_, const u16* gw_, u16* out_, char* ws_
    ) {
  __shared__ __attribute__((aligned(1024))) char smem[163840];
  LAS char* L = (LAS char*)smem;
  u64 pt0 = 0, pt1 = 0;
  STAMP(0);
  const u32 tid = threadIdx.x, lane = tid & 63, w = __builtin_amdgcn_readfirstlane(tid >> 6);
  const u32 blk = blockIdx.x, xc = blk & 7, ii = blk >> 3;
  v16u ka; v8u kb; v2u kc;
  u64 pc0; u32 t0_, t1_;
  asm volatile(
      "s_load_dwordx16 %0, %6, 0x0\n\ts_load_dwordx8 %1, %6, 0x40\n\ts_load_dwordx2 %2, %6, 0x60\n\t"
      "s_getpc_b64 %3\n.Lpf_begin:\n\t"
      "s_min_u32 %4, %7, .Lpf_end-.Lpf_begin\n\ts_load_dword %4, %3, %4\n\t"
      "s_waitcnt lgkmcnt(0)"
      : "=&s"(ka), "=&s"(kb), "=&s"(kc), "=&s"(pc0), "=&s"(t0_), "=&s"(t1_)
      : "s"(__builtin_amdgcn_kernarg_segment_ptr()), "s"((ii * 8 + w) * 64)
      : "memory", "scc");
  const GAS u16* x = (const GAS u16*)ptr(ka[0], ka[1]);
  const u64 ckv = ptr(ka[2], ka[3]), wq = ptr(ka[4], ka[5]), wuk = ptr(ka[6], ka[7]), wuv = ptr(ka[8], ka[9]), wo = ptr(ka[10], ka[11]), w1 = ptr(ka[12], ka[13]);
  const GAS u16* b1 = (const GAS u16*)ptr(ka[14], ka[15]);
  const u64 w2 = ptr(kb[0], kb[1]), gw = ptr(kb[4], kb[5]);
  const GAS u16* b2 = (const GAS u16*)ptr(kb[2], kb[3]);
  GAS u16* out = (GAS u16*)ptr(kb[6], kb[7]);
  const u64 ws = ptr(kc[0], kc[1]);
  const u32 lr = lane & 15, lg = lane >> 4;
  const __amdgpu_buffer_rsrc_t rws = mk_rsrc(ws, WS_END);
  const u32 epoch = *(const CAS u32*)(ws + WS_E);
  auto get_tag = [&]() { u32 t = epoch; asm volatile("s_add_u32 %0, %0, 1" : "+s"(t)::"scc"); return t; };
  const bool g2role = ii < 16;
  const u32 k = ii & 15;
  const u32 ha = 4 * xc + (ii >> 3), jm = ii & 7;
  const u32 h = 4 * xc + (k >> 2), j = k & 3;
  const u32 g1 = 16 * xc + k, hg = g1 & 1, tile = g1 >> 1, b = tile >> 5;
  const v4u sck = mk_srd(ckv, 2 * 4096 * 1024);
  auto ck_dma = [&](u32 i0, u32 i1) {
#pragma nounroll
    for (u32 i = i0; i < i1; ++i) {
      const u32 row = 16 * w + (i ^ (8 * hg));
      dma(sck, (lane * 16) ^ ((((row & 3) << 2) | ((row >> 2) & 3)) << 4), (tile * 128 + row) * 1024, row * 1024);
    }
  };
  if (w == 0) {
    v4u wqv[18], xa[18];
    v3u xs[3];
    const __amdgpu_buffer_rsrc_t rx = mk_rsrc((u64)x, 2304), rq = mk_rsrc(wq, KNOB(8, 0) & 4 ? 0 : 576 * 8192);
    const u32 xo = ((lr >> 3) * 576 + 72 * jm + 18 * lg) * 2;
#pragma unroll
    for (int i = 0; i < 3; ++i) xs[i] = __builtin_amdgcn_raw_buffer_load_b96(rx, xo + 12 * i, 0, 0);
    const u32 vo = (72 * jm + 18 * lg) * 8192 + ha * 256 + lr * 16;
#pragma unroll
    for (int s = 0; s < 18; ++s) { wqv[s] = __builtin_amdgcn_raw_buffer_load_b128(rq, vo, s * 8192, NT); SB(); }
    RAWBAR();
    const u32 di = (lr & 7) >> 1, sh = (lr & 1) * 16;
    u32 se[4], so[4];
#pragma unroll
    for (int i = 0; i < 4; ++i) {
      se[i] = di == (u32)i ? ((0x0100u << sh) | (0x0c0c0000u >> sh)) : 0x0c0c0c0cu;
      so[i] = di == (u32)i ? ((0x0302u << sh) | (0x0c0c0000u >> sh)) : 0x0c0c0c0cu;
    }
#pragma unroll
    for (int s = 0; s < 18; ++s)
#pragma unroll
      for (int i = 0; i < 4; ++i) xa[s][i] = __builtin_amdgcn_perm(0u, xs[s / 6][(s % 6) >> 1], (s & 1) ? so[i] : se[i]);
    u32 qo = WS_QP + (ha * 8 + jm) * 1024 + lane * 16;
    PIN(qo);
    const u32 tag = get_tag();
    SB();
    v4f acc = {0.f, 0.f, 0.f, 0.f};
#pragma unroll
    for (int s = 0; s < 18; ++s) acc = mfma(xa[s], wqv[s], acc);
    __builtin_amdgcn_raw_buffer_store_b128(tagged(acc, tag), rws, qo, 0, SC1);
  } else {
    RAWBAR();
    if (g2role) {
      SLEEP(0, K_SLEEP_UK);
      const v4u suk = mk_srd(wuk, 512 * 8192);
#pragma nounroll
      for (u32 n = w == 1 ? 0 : 4 * w; n < 4 * w + (KNOB(8, 0) & 8 ? 0 : 4); ++n) {
        const u32 row = 4 * (n & 3) + lg;
        dma(suk, row * 8192 + ((lr ^ row) << 4), (128 * j + 4 * (n & ~3u)) * 8192 + h * 256, L2_UK + n * 1024);
      }
      if (w == 7) {
        const v4u sx = mk_srd((u64)x, 2304), sg = mk_srd(gw, 1152);
        dma(sx, lane * 16, 0, L2_X); dma(sx, lane * 16, 1024, L2_X + 1024); dma(sx, lane * 16, 2048, L2_X + 2048);
        dma(sg, lane * 16, 0, L2_GW); dma(sg, lane * 16, 1024, L2_GW + 1024);
      }
    } else {
      ck_dma(0, KNOB(2, K_EARLY_CK));
      SLEEP(1, K_SLEEP_CK);
    }
  }
  STAMP(1);
  const u32 q4 = (lane >> 2) & 3, p4 = lane & 3;
  const u32 fr0 = 4 * (lg & 1) + q4, fr1 = fr0 ^ 4, fpc = 2 * (w & 3) + (p4 >> 1);
  const u32 fa0 = fr0 * 128 + ((fpc ^ (fr0 & 6)) << 4) + 8 * (p4 & 1), fa1 = fr1 * 128 + ((fpc ^ (fr1 & 6)) << 4) + 8 * (p4 & 1);
  const u32 rr = lane >> 3, pcs = ((lane & 7) ^ (rr & 6)) << 4;
  auto frag = [&](u32 a) { return cat(ldstr(L, a + fa0), ldstr(L, a + fa1)); };

  if (g2role) {
    STAMP(14);
    const bool reducer = k < 9;
    const u32 rho = 9 * xc + k, rmt = rho >> 1, rb_ = rho & 1;
    u32 b2v = 0;
    if (reducer && tid < 16 && !(KNOB(8, 0) & 256)) b2v = b2[8 * rho + (tid >> 1)];
    u32 aqp = WS_QP + (h * 8 + w) * 1024 + lane * 16, abq = L2_BQ + ((lg >> 1) * 8 + w) * 272 + (8 * lr + 4 * (lg & 1)) * 2;
    u32 auk = L2_UK + w * 4096 + lr * 256, abr = lr * 272 + 16 * lg, aqg = WS_QG + ((lg >> 1) * 32 + h) * 2048 + (32 * j + 4 * w + (lr >> 2)) * 16;
    u32 auk0 = auk + ((lg ^ lr) << 4), auk1 = auk + (((4 + lg) ^ lr) << 4), auk2 = auk + (((8 + lg) ^ lr) << 4), auk3 = auk + (((12 + lg) ^ lr) << 4);
    PIN(aqp); PIN(abq); PIN(abr); PIN(aqg); PIN(auk0); PIN(auk1); PIN(auk2); PIN(auk3);
    STAMP(15);
    RAWBAR();
    STAMP(13);
    const u32 tag = get_tag();
    SLEEP(3, K_SLEEP_1A);
    v4u qp;
#pragma nounroll
    for (u32 sp = 0; sp < SPIN_CAP; ++sp) {
      ORDER();
      qp = ld16(rws, aqp);
      COUNT(0);
      if (__all(tagok(qp, tag))) break;
    }
    *(LAS v2u*)(L + abq) = v2u{qp.x, qp.z};
    PIN(b2v);
    VMW(0);
    STAMP(2);
    BAR();
    {
      const v4u a0 = lds128(L, auk0), b0 = lds128(L, L2_BQ + abr), a1 = lds128(L, auk1), b1_ = lds128(L, L2_BQ + abr + 64);
      const v4u a2 = lds128(L, auk2), b2_ = lds128(L, L2_BQ + abr + 128), a3 = lds128(L, auk3), b3 = lds128(L, L2_BQ + abr + 192);
      SB();
      v4f acc = {0.f, 0.f, 0.f, 0.f};
      acc = mfma(b0, a0, acc); acc = mfma(b1_, a1, acc); acc = mfma(b2_, a2, acc); acc = mfma(b3, a3, acc);
      const float sc = 0.08838834764831845f * 1.4426950408889634f;
      const float v = add16((acc[0] + acc[1]) + (acc[2] + acc[3])) * sc;
      const u32 p01 = pk(v, dpp<0xB1>(v));
      const u32 p23 = __builtin_amdgcn_update_dpp(0, (int)p01, 0x4E, 0xf, 0xf, true);
      if ((lane & 19) == 0) __builtin_amdgcn_raw_buffer_store_b128(v4u{p01, tag, p23, tag}, rws, aqg, 0, SC1);
    }
    STAMP(3);
    BAR();
    SLEEP(4, K_SLEEP_S3);
    const v4u suv = mk_srd(wuv, 512 * 8192), swo = mk_srd(wo, 4096 * 1152);
#pragma nounroll
    for (u32 slot = w; slot < 32; slot += 8)
      dma(suv, (16 * (rr & 3) + (rr >> 2)) * 8192 + pcs, (128 * j + 64 * (slot >> 4) + (slot & 14)) * 8192 + h * 256 + (slot & 1) * 128, L2_UV + slot * 1024);
    auto wo_dma = [&](u32 s0, u32 s1) {
#pragma nounroll
      for (u32 slot = s0; slot < s1; slot += 8) {
        const u32 rbl = (slot * 57) >> 9, cg = slot - 9 * rbl;
        dma(swo, rr * 1152 + pcs, (h * 128 + 8 * ((rbl + 4 * j) & 15)) * 1152 + cg * 128, slot * 1024);
      }
    };
    wo_dma(w, 48);
    VMW(6);
    BAR();
    v4u uvA[4];
#pragma unroll
    for (int s = 0; s < 4; ++s) uvA[s] = frag(L2_UV + ((4 * s + lg) * 2 + (w >> 2)) * 1024);
    float gate0 = 0.f, gate1 = 0.f;
    if (reducer) {
      float a0 = 0.f, a1 = 0.f;
#pragma unroll
      for (int i = 0; i < 5; ++i) {
        const u32 e = lane + 64 * i;
        const u32 x0 = *(LAS u32*)(L + L2_X + e * 4), x1 = *(LAS u32*)(L + L2_X + 1152 + e * 4), gv = *(LAS u32*)(L + L2_GW + e * 4);
        a0 += e < 288 ? bflo(x0) * bflo(gv) + bfhi(x0) * bfhi(gv) : 0.f;
        a1 += e < 288 ? bflo(x1) * bflo(gv) + bfhi(x1) * bfhi(gv) : 0.f;
      }
      gate0 = 1.f / (1.f + __expf(-add32(add16(sum16(a0)))));
      gate1 = 1.f / (1.f + __expf(-add32(add16(sum16(a1)))));
    }
    BAR();
    wo_dma(48 + w, 144);
    STAMP(4);
    u32 sto = (lane * 2 + (h >> 4)) * 128 + (h & 15) * 8;
    u32 ogo = WS_OG + ((h * 4 + j) * 64 + 8 * w + lg) * 512 + lr * 16;
    u32 awo = lg * 9216 + (w >> 2) * 1024, af = L2_F + (8 * w + lg) * 4;
    const u32 U1 = 16 * (lg & 1) + lr, U2 = 4 * w + (lr >> 2);
    u32 abc = L2_BC + ((w >> 2) * 8 + (w & 3) * 2 + (lg >> 1)) * 272 + (U1 ^ ((U1 >> 1) & 1)) * 8;
    u32 act = L2_CT + (lg >> 1) * 272 + (U2 ^ ((U2 >> 1) & 1)) * 8 + (lr & 3) * 2, ail = L2_IL + (lg >> 1) * 4;
    u32 acr[4];
#pragma unroll
    for (int s = 0; s < 4; ++s) { acr[s] = L2_CT + (lr & 1) * 272 + (32 * ((s + j) & 3) + 8 * lg) * 2; PIN(acr[s]); }
    u32 fo = WS_FG + (128 + h * 4 + j) * 128 + lr * 8 + (w + 8 * lg) * 32768, fro = WS_FG + rmt * 32768 + (tid >> 2) * 128 + rb_ * 64 + (tid & 3) * 16;
    u32 sel[4];
    {
      const u32 di = (lr & 7) >> 1, sh = (lr & 1) * 16, src = (lr >> 3) ? 0x0302u : 0x0100u;
#pragma unroll
      for (int i = 0; i < 4; ++i) { sel[i] = di == (u32)i ? ((src << sh) | (0x0c0c0000u >> sh)) : 0x0c0c0c0cu; PIN(sel[i]); }
    }
    PIN(sto); PIN(ogo); PIN(awo); PIN(af); PIN(abc); PIN(act); PIN(ail); PIN(fo); PIN(fro); PIN(gate0); PIN(gate1);
    VMW(0);
    STAMP(5);
    float fch = 0.f;
    if (w == 0) {
      v2u mv;
#pragma nounroll
      for (u32 sp = 0; sp < SPIN_CAP; ++sp) {
        ORDER();
        mv = ld8(rws, WS_ST + sto);
        COUNT(1);
        if (__all(mv.y == tag)) break;
      }
      float M = f_(mv.x);
      M = fmaxf(M, dpp<0xB1>(M)); M = fmaxf(M, dpp<0x4E>(M)); M = fmaxf(M, dpp<0x141>(M)); M = fmaxf(M, dpp<0x140>(M)); M = max16(M);
      fch = __builtin_amdgcn_exp2f(f_(mv.x) - M);
      const u32 hi = pk(fch, 0.f) & 0xffffu;
      *(LAS u32*)(L + L2_F + lane * 4) = pk(fch, fch - bflo(hi));
    }
    BAR();
    SLEEP(5, K_SLEEP_O);
    STAMP(6);
    v4u og[4];
    v2u lv = {0u, tag};
#pragma unroll
    for (int e = 0; e < 4; ++e) og[e] = ld16(rws, ogo + (e >> 1) * 2048 + (e & 1) * 256);
    if (w == 0) lv = ld8(rws, WS_SL + sto);
    ORDER();
    v4u woA[5][4], fA[2];
#pragma unroll
    for (int e = 0; e < 2; ++e) {
      const u32 fv = *(LAS u32*)(L + af + 16 * e);
#pragma unroll
      for (int i = 0; i < 4; ++i) fA[e][i] = __builtin_amdgcn_perm(0u, fv, sel[i]);
    }
#pragma unroll
    for (int n = 0; n < 4; ++n)
#pragma unroll
      for (int s = 0; s < 4; ++s) woA[n][s] = frag(awo + s * 36864 + n * 2048);
#pragma unroll
    for (int s = 0; s < 4; ++s) woA[4][s] = frag(lg * 9216 + s * 36864 + 8192);
    ORDER();
#pragma nounroll
    for (u32 sp = 0; sp < SPIN_CAP; ++sp) {
      const bool k0 = __all(tagok(og[0], tag) & tagok(og[1], tag)), k1 = __all(tagok(og[2], tag) & tagok(og[3], tag) & (lv.y == tag));
      if (k0 & k1) break;
      COUNT(2);
      ORDER();
      if (!k0) { og[0] = ld16(rws, ogo); og[1] = ld16(rws, ogo + 256); }
      if (!k1) { og[2] = ld16(rws, ogo + 2048); og[3] = ld16(rws, ogo + 2304); if (w == 0) lv = ld8(rws, WS_SL + sto); }
    }
    STAMP(7);
    {
      v4f acc = {0.f, 0.f, 0.f, 0.f};
      acc = mfma(fA[0], v4u{og[0].x, og[0].z, og[1].x, og[1].z}, acc);
      acc = mfma(fA[1], v4u{og[2].x, og[2].z, og[3].x, og[3].z}, acc);
      *(LAS v2u*)(L + abc) = v2u{pk(acc[0], acc[1]), pk(acc[2], acc[3])};
      if (w == 0) {
        const float ls = add16(sum16(fch * f_(lv.x)));
        if ((lane & 31) == 0) *(LAS float*)(L + L2_IL + (lane >> 5) * 4) = 1.f / ls;
      }
    }
    BAR();
    {
      const v4u c0 = lds128(L, L2_BC + abr), c1 = lds128(L, L2_BC + abr + 64), c2 = lds128(L, L2_BC + abr + 128), c3 = lds128(L, L2_BC + abr + 192);
      const float il = *(LAS float*)(L + ail);
      SB();
      v4f acc = {0.f, 0.f, 0.f, 0.f};
      acc = mfma(c0, uvA[0], acc); acc = mfma(c1, uvA[1], acc); acc = mfma(c2, uvA[2], acc); acc = mfma(c3, uvA[3], acc);
      const float v = add16((acc[0] + acc[1]) + (acc[2] + acc[3])) * il;
      if (!(lg & 1)) *(LAS u16*)(L + act) = (u16)pk(v, v);
    }
    BAR();
    STAMP(8);
    {
      v4u cb[4];
#pragma unroll
      for (int s = 0; s < 4; ++s) cb[s] = lds128(L, acr[s]);
      SB();
      v4f acc[5];
#pragma unroll
      for (int n = 0; n < 5; ++n) acc[n] = v4f{0.f, 0.f, 0.f, 0.f};
#pragma unroll
      for (int n = 0; n < 4; ++n)
#pragma unroll
        for (int s = 0; s < 4; ++s) acc[n] = mfma(cb[s], woA[n][s], acc[n]);
      __builtin_amdgcn_raw_buffer_store_b64(v2u{sel4(acc[0], acc[1], acc[2], acc[3], lg), tag}, rws, fo, 0, SC1);
      if (w < 4) {
#pragma unroll
        for (int s = 0; s < 4; ++s) acc[4] = mfma(cb[s], woA[4][s], acc[4]);
        if (lane < 16) __builtin_amdgcn_raw_buffer_store_b64(v2u{pk(acc[4][0], acc[4][1]), tag}, rws, fo, 32 * 32768, SC1);
      }
    }
    STAMP(9);
    if (reducer) {
      SLEEP(7, K_SLEEP_F);
      v4u pm = ld16(rws, fro), pa = ld16(rws, fro + 16384);
#pragma nounroll
      for (u32 sp = 0; sp < SPIN_CAP; ++sp) {
        const bool k0 = __all(tagok(pm, tag)), k1 = __all(tagok(pa, tag));
        if (k0 & k1) break;
        COUNT(3);
        ORDER();
        if (!k0) pm = ld16(rws, fro);
        if (!k1) pa = ld16(rws, fro + 16384);
      }
      STAMP(10);
      v4f v = {gate0 * bflo(pa.x) + (1.f - gate0) * bflo(pm.x), gate1 * bfhi(pa.x) + (1.f - gate1) * bfhi(pm.x),
               gate0 * bflo(pa.z) + (1.f - gate0) * bflo(pm.z), gate1 * bfhi(pa.z) + (1.f - gate1) * bfhi(pm.z)};
#pragma unroll
      for (int i = 0; i < 4; ++i) {
        float t = v[i];
        t += dpp<0x124>(t); t += dpp<0x128>(t);
        v[i] = add32(add16(t));
      }
      if (lane < 4) *(LAS v4f*)(L + L2_RD + w * 64 + lane * 16) = v;
      BAR();
      if (tid < 16) {
        float s = (1.f - (tid & 1 ? gate1 : gate0)) * bflo(b2v);
#pragma unroll
        for (int i = 0; i < 8; ++i) s += *(LAS float*)(L + L2_RD + i * 64 + tid * 4);
        out[(tid & 1) * 576 + 8 * rho + (tid >> 1)] = (u16)pk(s, 0.f);
      }
      if (blk == 0 && tid == 0) __hip_atomic_store((GAS u32*)(ws + WS_E), tag, __ATOMIC_RELAXED, __HIP_MEMORY_SCOPE_AGENT);
      STAMP(11);
    }
  } else {
    const u32 hu = tid >> 4, hf = 2 * hu + ((lr >> 1) & 1);
    u32 b1v = 0, xv = 0;
    if (tid < 288 && lr < 4 && !(KNOB(8, 0) & 512)) b1v = b1[36 * g1 + hf];
    if (tid < 128 && lane < 36 && !(KNOB(8, 0) & 512)) xv = x[w * 576 + 36 * k + lane];
    if (KNOB(9, K_CK_BAR)) RAWBAR();
    ck_dma(w == 0 ? 0 : KNOB(2, K_EARLY_CK), 16);
    STAMP(2);
    const u32 sg = (0x9cu >> (2 * lg)) & 3;
    const u32 pir = ((lr & 3) << 2) | (lr >> 2);
    u32 qo = WS_QG + (b * 32 + hg * 16) * 2048 + tid * 16;
    u32 akr[4], aqr[4], aqw[4];
#pragma unroll
    for (int s = 0; s < 4; ++s) {
      akr[s] = (16 * w + lr) * 1024 + (((4 * s + sg) ^ pir) << 4);
      aqr[s] = L1_Q + lr * 1024 + (((4 * s + sg) ^ lr) << 4);
      const u32 head = 4 * s + (w >> 1), c4 = tid & 127;
      aqw[s] = L1_Q + head * 1024 + (((c4 >> 1) ^ head) << 4) + (c4 & 1) * 8;
      PIN(akr[s]); PIN(aqr[s]); PIN(aqw[s]);
    }
    u32 amx = L1_MX + lr * 32, apw = L1_P + lr * 272 + (16 * w + 4 * lg) * 2, apr = L1_P + lr * 272 + 16 * lg;
    u32 oo = WS_OG + (((hg * 16 + 4 * lg) * 4 + (w >> 1)) * 64 + tile) * 512 + (w & 1) * 256 + lr * 16, sto = g1 * 128 + lr * 8;
    PIN(qo); PIN(amx); PIN(apw); PIN(apr); PIN(oo); PIN(sto);
    VMW(0);
    const u32 tag = get_tag();
    PIN(b1v); PIN(xv);
    STAMP(3);
    v4u qg[4];
#pragma unroll
    for (int u = 0; u < 4; ++u) qg[u] = ld16(rws, qo + u * 8192);
    ORDER();
    v4u kA[16];
#pragma unroll
    for (int s = 0; s < 16; ++s) kA[s] = lds128(L, akr[s & 3] + (s >> 2) * 256);
    if (tid < 128) {
      const u32 U = lane >> 2;
      *(LAS u16*)(L + L1_XB + w * 128 + (U ^ ((U >> 1) & 1)) * 8 + (lane & 3) * 2) = (u16)xv;
      *(LAS u16*)(L + L1_HB + tid * 2) = 0;
    }
    BAR();
    v4u vA[4][4];
#pragma unroll
    for (int tt = 0; tt < 4; ++tt) {
      const u32 a0 = (8 * lg + q4) * 1024 + (((8 * w + 2 * tt + (p4 >> 1)) ^ ((q4 << 2) | (2 * (lg & 1)))) << 4) + 8 * (p4 & 1);
      const u32 a1 = (a0 ^ 16) + 4096;
#pragma unroll
      for (int t = 0; t < 4; ++t) vA[tt][t] = cat(ldstr(L, a0 + t * 32768), ldstr(L, a1 + t * 32768));
    }
    ORDER();
#pragma nounroll
    for (u32 sp = 0; sp < SPIN_CAP; ++sp) {
      const bool k0 = __all(tagok(qg[0], tag)), k1 = __all(tagok(qg[1], tag)), k2 = __all(tagok(qg[2], tag)), k3 = __all(tagok(qg[3], tag));
      if (k0 & k1 & k2 & k3) break;
      COUNT(0);
      ORDER();
      if (!k0) qg[0] = ld16(rws, qo);
      if (!k1) qg[1] = ld16(rws, qo + 8192);
      if (!k2) qg[2] = ld16(rws, qo + 16384);
      if (!k3) qg[3] = ld16(rws, qo + 24576);
    }
#pragma unroll
    for (int u = 0; u < 4; ++u) *(LAS v2u*)(L + aqw[u]) = v2u{qg[u].x, qg[u].z};
    STAMP(4);
    BAR();
    const v4u sw1 = mk_srd(w1, 576 * 9216), sw2 = mk_srd(w2, 4608 * 1152);
    auto w_dma1 = [&](v4u srd, u32 pitch, u32 base, u32 lds, u32 slot) {
      const u32 rbl = (slot * 57) >> 9, cg = slot - 9 * rbl;
      dma(srd, (slot >= 36 && rr >= 4 ? 0x80000000u : rr * pitch) + pcs, base + 8 * rbl * pitch + cg * 128, lds + slot * 1024);
    };
    auto w_dma = [&](v4u srd, u32 pitch, u32 base, u32 lds) {
#pragma nounroll
      for (u32 slot = w; slot < 45; slot += 8) w_dma1(srd, pitch, base, lds, slot);
    };
    v4f S = {0.f, 0.f, 0.f, 0.f};
    {
      v4u qB[16];
#pragma unroll
      for (int s = 0; s < 16; ++s) qB[s] = lds128(L, aqr[s & 3] + (s >> 2) * 256);
      SB();
#pragma unroll
      for (int s = 0; s < 16; ++s) {
        S = mfma(kA[s], qB[s], S);
      }
    }
    {
      const float mw = max32(max16(fmaxf(fmaxf(S[0], S[1]), fmaxf(S[2], S[3]))));
      if (lane < 16) *(LAS float*)(L + amx + w * 4) = mw;
    }
    BAR();
    STAMP(5);
    {
      const v4f m0 = *(LAS v4f*)(L + amx), m1 = *(LAS v4f*)(L + amx + 16);
      const float M = fmaxf(fmaxf(fmaxf(m0[0], m0[1]), fmaxf(m0[2], m0[3])), fmaxf(fmaxf(m1[0], m1[1]), fmaxf(m1[2], m1[3])));
      const float p0 = __builtin_amdgcn_exp2f(S[0] - M), p1 = __builtin_amdgcn_exp2f(S[1] - M), p2 = __builtin_amdgcn_exp2f(S[2] - M), p3 = __builtin_amdgcn_exp2f(S[3] - M);
      *(LAS v2u*)(L + apw) = v2u{pk(p0, p1), pk(p2, p3)};
      if (tid < 16) __builtin_amdgcn_raw_buffer_store_b64(v2u{u_(M), tag}, rws, sto, WS_ST, SC1);
    }
    BAR();
    {
      v4u pB[4];
#pragma unroll
      for (int t = 0; t < 4; ++t) pB[t] = lds128(L, apr + 64 * t);
      SB();
      v4f o[4];
#pragma unroll
      for (int tt = 0; tt < 4; ++tt) o[tt] = v4f{0.f, 0.f, 0.f, 0.f};
#pragma unroll
      for (int t = 0; t < 4; ++t)
#pragma unroll
        for (int tt = 0; tt < 4; ++tt) o[tt] = mfma(pB[t], vA[tt][t], o[tt]);
#pragma unroll
      for (int r = 0; r < 4; ++r)
        __builtin_amdgcn_raw_buffer_store_b128(v4u{pk(o[0][r], o[1][r]), tag, pk(o[2][r], o[3][r]), tag}, rws, oo, r * 4 * 64 * 512, SC1);
      if (w == 7) {
        v4f acc = {0.f, 0.f, 0.f, 0.f};
        const v4u ones = {0x3f803f80u, 0x3f803f80u, 0x3f803f80u, 0x3f803f80u};
#pragma unroll
        for (int t = 0; t < 4; ++t) acc = mfma(ones, pB[t], acc);
        if (lane < 16) __builtin_amdgcn_raw_buffer_store_b64(v2u{u_(acc[0]), tag}, rws, sto, WS_SL, SC1);
      }
    }
    STAMP(6);
    w_dma(sw1, 9216, 36 * k * 9216 + xc * 1152, L1_W1);
    w_dma(sw2, 1152, 36 * g1 * 1152, L1_W2);
    auto gemv36 = [&](u32 lw, u32 lb, u32 vo, u32 stride) {
      const v4u h0 = lds128(L, lb + (lr & 1) * 128 + 16 * lg), h1 = lds128(L, lb + (lr & 1) * 128 + 64 + 16 * lg);
      v4u a0[5], a1[5];
#pragma unroll
      for (int n = 0; n < 5; ++n) {
        const u32 cg = n < 4 ? 2 * n + (w >> 2) : 8;
        a0[n] = frag(lw + (lg * 9 + cg) * 1024);
        a1[n] = frag(lw + (36 + cg) * 1024);
      }
      SB();
      v4f acc[5];
#pragma unroll
      for (int n = 0; n < 5; ++n) acc[n] = mfma(h1, a1[n], mfma(h0, a0[n], v4f{0.f, 0.f, 0.f, 0.f}));
      __builtin_amdgcn_raw_buffer_store_b64(v2u{sel4(acc[0], acc[1], acc[2], acc[3], lg), tag}, rws, vo, 0, SC1);
      if (w < 4 && lane < 16) __builtin_amdgcn_raw_buffer_store_b64(v2u{pk(acc[4][0], acc[4][1]), tag}, rws, vo, 32 * stride, SC1);
    };
    if (w < 5) { VMW(6); } else { VMW(5); }
    BAR();
    STAMP(7);
    gemv36(L1_W1, L1_XB, WS_HG + g1 * 4608 + (w + 8 * lg) * 128 + lr * 8, 128);
    STAMP(8);
    SLEEP(6, K_SLEEP_H);
    if (w < 5) {
      const u32 ho = WS_HG + (16 * xc + lr) * 4608 + (18 * k + min(hu, 17u)) * 16;
      v4u hp;
#pragma nounroll
      for (u32 sp = 0; sp < SPIN_CAP; ++sp) {
        ORDER();
        hp = ld16(rws, ho);
        COUNT(1);
        if (__all(tagok(hp, tag))) break;
      }
      const float s0 = sum16(bflo(hp.x)), s1 = sum16(bfhi(hp.x)), s2 = sum16(bflo(hp.z)), s3 = sum16(bfhi(hp.z));
      const float v = (lr == 0 ? s0 : lr == 1 ? s1 : lr == 2 ? s2 : s3) + bflo(b1v);
      const float u2 = 1.5957691216057308f * (v + 0.044715f * v * v * v);
      const float hv = v / (1.f + __expf(-u2));
      if (tid < 288 && lr < 4) *(LAS u16*)(L + L1_HB + (lr & 1) * 128 + ((hf >> 2) ^ ((hf >> 3) & 1)) * 8 + (hf & 3) * 2) = (u16)pk(hv, 0.f);
    }
    VMW(0);
    STAMP(9);
    BAR();
    gemv36(L1_W2, L1_HB, WS_FG + g1 * 128 + lr * 8 + (w + 8 * lg) * 32768, 32768);
    STAMP(10);
  }
  STAMP_OUT();
  asm volatile(".Lpf_end:");
}

extern "C" void kernel_launch(void* const* d_in, const int* in_sizes, int n_in, void* d_out, int out_size, void* d_ws, size_t ws_size, hipStream_t stream) {
  fused_decode<<<256, 512, 0, stream>>>((const u16*)d_in[0], (const u16*)d_in[1], (const u16*)d_in[2], (const u16*)d_in[3], (const u16*)d_in[4],
      (const u16*)d_in[5], (const u16*)d_in[7], (const u16*)d_in[8], (const u16*)d_in[9], (const u16*)d_in[10], (const u16*)d_in[11], (u16*)d_out, (char*)d_ws + WS_BASE(ws_size)
      );
}
```

```cpp
#define PROBE_ID 0
#define PROBE_BLK 0
#include <hip/hip_runtime.h>
#include <stdint.h>

typedef unsigned u32;
typedef unsigned long long u64;
typedef unsigned short u16;
typedef u32 v4u __attribute__((ext_vector_type(4)));
typedef u32 v3u __attribute__((ext_vector_type(3)));
typedef u32 v2u __attribute__((ext_vector_type(2)));
typedef float v4f __attribute__((ext_vector_type(4)));
typedef float v2f __attribute__((ext_vector_type(2)));
typedef short v8s __attribute__((ext_vector_type(8)));
typedef short v4s __attribute__((ext_vector_type(4)));
typedef __bf16 v2b __attribute__((ext_vector_type(2)));
#define LAS __attribute__((address_space(3)))
#define GAS __attribute__((address_space(1)))
#define CAS __attribute__((address_space(4)))
#define DI __device__ __forceinline__

#define K_PREFETCH 1
#define K_EARLY_CK 0
#define K_T1A 220
#define K_TP 90
#define K_TC 120
#define K_TQ 370
#define K_TM 560
#define K_TH 640
#define K_TF 780
#define K_SLEEP_UK 0
#define K_SLEEP_CK 0
#define K_CK_BAR 1
#define K_W1_EARLY 1
#define K_WS_END 0
#define WS_BASE(sz) (K_WS_END ? (((sz) - (16u << 20)) & ~(size_t)4095) : (size_t)0)
constexpr u32 WS_E = 0;
constexpr u32 WS_T0 = 1024;
constexpr u32 WS_QP = 4096;
constexpr u32 WS_QG = WS_QP + 32 * 8 * 1024;
constexpr u32 WS_ST = WS_QG + 2 * 32 * 2048;
constexpr u32 WS_SL = WS_ST + 128 * 128;
constexpr u32 WS_HG = WS_SL + 128 * 128;
constexpr u32 WS_FG = WS_HG + 128 * 2 * 2304;
constexpr u32 WS_OG = WS_FG + 72 * 256 * 64;
constexpr u32 WS_END = WS_OG + 32 * 4 * 64 * 512;
constexpr u32 WS_DBG = 8u << 20;
constexpr u32 L1_Q = 131072, L1_MX = L1_Q + 16384, L1_P = L1_MX + 512, L1_XB = L1_P + 4352, L1_HB = L1_XB + 256, L1_XR = L1_HB + 256, L1_B1 = L1_XR + 256, L1_T0 = L1_B1 + 256;
constexpr u32 L1_W1 = 0, L1_W2 = 46080;
constexpr u32 L2_UV = 114688, L2_UK = 81920;
constexpr u32 L2_BQ = 147456, L2_BC = L2_BQ + 4352, L2_CT = L2_BC + 4352, L2_F = L2_CT + 544, L2_IL = L2_F + 256, L2_RD = L2_IL + 16, L2_X = L2_RD + 512, L2_GW = L2_X + 2304, L2_B2 = L2_GW + 1280;
static_assert(L1_T0 + 16 <= 162816 && L2_B2 + 16 <= 162816, "lds");
static_assert(L1_XB % 16 == 0 && L2_X % 16 == 0 && L2_GW % 16 == 0 && L2_RD % 16 == 0, "align");
constexpr int SC1 = 16, NT = 2;
constexpr u32 SPIN_CAP = 1u << 15;

#define SLEEP(idx, dflt) do { if (dflt) __builtin_amdgcn_s_sleep(dflt); } while (0)
#define KNOB(idx, dflt) (dflt)
#define STAMP(i) do { if ((i) == 0) asm volatile("s_memrealtime %0\n\ts_waitcnt lgkmcnt(0)" : "=s"(pt0)::"memory"); if ((i) == PROBE_ID) asm volatile("s_memrealtime %0\n\ts_waitcnt lgkmcnt(0)" : "=s"(pt1)::"memory"); } while (0)
#define COUNT(i) do {} while (0)
#define STAMP_OUT() do { if (blk == PROBE_BLK && w == 0) { u64 t_; do { __builtin_amdgcn_s_sleep(2); asm volatile("s_memrealtime %0\n\ts_waitcnt lgkmcnt(0)" : "=s"(t_)::"memory"); } while (t_ - pt0 < 4000 + 4 * (pt1 - pt0)); } } while (0)

DI v4u mk_srd(u64 a, u32 bytes) {
  return v4u{(u32)a, (u32)(a >> 32) & 0xffffu, bytes, 0x00020000u};
}
DI __amdgpu_buffer_rsrc_t mk_rsrc(u64 p, u32 bytes) { return __builtin_amdgcn_make_buffer_rsrc((void*)p, 0, (int)bytes, 0x00020000); }
DI void dma(v4u srd, u32 voff, u32 soff, u32 lds) {
  asm volatile("s_mov_b32 m0, %3\n\ts_nop 0\n\tbuffer_load_dwordx4 %0, %1, %2 offen nt lds" ::"v"(voff), "s"(srd), "s"(soff), "s"(lds) : "memory");
}
DI void dma4(v4u srd, u32 voff, u32 soff, u32 lds) {
  asm volatile("s_mov_b32 m0, %3\n\ts_nop 0\n\tbuffer_load_dword %0, %1, %2 offen nt lds" ::"v"(voff), "s"(srd), "s"(soff), "s"(lds) : "memory");
}
#define BAR() asm volatile("s_waitcnt lgkmcnt(0)\n\ts_barrier" ::: "memory")
#define RAWBAR() asm volatile("s_barrier" ::: "memory")
#define VMW(n) asm volatile("s_waitcnt vmcnt(" #n ")" ::: "memory")
#define ORDER() asm volatile("" ::: "memory")
#define PIN(x) asm volatile("" : "+v"(x))
#define SB() __builtin_amdgcn_sched_barrier(0)
DI v4f mfma(v4u a, v4u b, v4f c) { return __builtin_amdgcn_mfma_f32_16x16x32_bf16(__builtin_bit_cast(v8s, a), __builtin_bit_cast(v8s, b), c, 0, 0, 0); }
DI u32 pk(float a, float b) { return __builtin_bit_cast(u32, __builtin_convertvector(v2f{a, b}, v2b)); }
DI float bflo(u32 x) { return __builtin_bit_cast(float, x << 16); }
DI float bfhi(u32 x) { return __builtin_bit_cast(float, x & 0xffff0000u); }
DI float f_(u32 x) { return __builtin_bit_cast(float, x); }
DI u32 u_(float x) { return __builtin_bit_cast(u32, x); }
template <int C> DI float dpp(float x) { return f_((u32)__builtin_amdgcn_update_dpp(0, (int)u_(x), C, 0xf, 0xf, true)); }
DI float sum8(float x) { x += dpp<0xB1>(x); x += dpp<0x4E>(x); x += dpp<0x141>(x); return x; }
DI float sum16(float x) { x = sum8(x); x += dpp<0x140>(x); return x; }
DI float add16(float x) { auto r = __builtin_amdgcn_permlane16_swap(u_(x), u_(x), false, false); return f_(r[0]) + f_(r[1]); }
DI float add32(float x) { auto r = __builtin_amdgcn_permlane32_swap(u_(x), u_(x), false, false); return f_(r[0]) + f_(r[1]); }
DI float max16(float x) { auto r = __builtin_amdgcn_permlane16_swap(u_(x), u_(x), false, false); return fmaxf(f_(r[0]), f_(r[1])); }
DI float max32(float x) { auto r = __builtin_amdgcn_permlane32_swap(u_(x), u_(x), false, false); return fmaxf(f_(r[0]), f_(r[1])); }
DI v4u lds128(LAS char* L, u32 a) { return *(LAS v4u*)(L + a); }
DI v2u ldstr(LAS char* L, u32 a) { return __builtin_bit_cast(v2u, __builtin_amdgcn_ds_read_tr16_b64_v4i16((LAS v4s*)(L + a))); }
DI v4u cat(v2u a, v2u b) { return v4u{a.x, a.y, b.x, b.y}; }
DI bool tagok(v4u a, u32 tag) { return (a.y == tag) & (a.w == tag); }
DI v4u tagged(v4f a, u32 tag) { return v4u{pk(a[0], a[1]), tag, pk(a[2], a[3]), tag}; }
DI u32 sel4(v4f a0, v4f a1, v4f a2, v4f a3, u32 g) {
  u32 p0 = pk(a0[0], a0[1]), p1 = pk(a1[0], a1[1]), p2 = pk(a2[0], a2[1]), p3 = pk(a3[0], a3[1]);
  p0 = g & 1 ? p1 : p0;
  p2 = g & 1 ? p3 : p2;
  return g & 2 ? p2 : p0;
}
DI u32 now() { u64 t; asm volatile("s_memrealtime %0\n\ts_waitcnt lgkmcnt(0)" : "=s"(t)::"memory"); return (u32)t; }
DI void wait_until(u32 t) { while ((int)(now() - t) < 0) __builtin_amdgcn_s_sleep(1); }
DI bool start_ok(u32 e, u32 mine) { return e - mine + 300u <= 600u; }
DI u32 latest_start(u32 e, u32 mine) {
  int d = start_ok(e, mine) ? (int)(e - mine) : 0;
  d = max(d, __builtin_amdgcn_update_dpp(0, d, 0xB1, 0xf, 0xf, true));
  d = max(d, __builtin_amdgcn_update_dpp(0, d, 0x4E, 0xf, 0xf, true));
  d = max(d, __builtin_amdgcn_update_dpp(0, d, 0x141, 0xf, 0xf, true));
  return mine + (u32)__builtin_amdgcn_readfirstlane(d);
}
DI u32 ld4(__amdgpu_buffer_rsrc_t r, u32 o) { return __builtin_amdgcn_raw_buffer_load_b32(r, o, 0, SC1); }
DI v4u ld16(__amdgpu_buffer_rsrc_t r, u32 o) { return __builtin_amdgcn_raw_buffer_load_b128(r, o, 0, SC1); }
DI v2u ld8(__amdgpu_buffer_rsrc_t r, u32 o) { return __builtin_amdgcn_raw_buffer_load_b64(r, o, 0, SC1); }

typedef u32 v16u __attribute__((ext_vector_type(16)));
typedef u32 v8u __attribute__((ext_vector_type(8)));
DI u64 ptr(u32 lo, u32 hi) { return (u64)lo | ((u64)hi << 32); }

__global__ void __launch_bounds__(512, 2) fused_decode(const u16* x_, const u16* ckv_, const u16* wq_, const u16* wuk_, const u16* wuv_, const u16* wo_,
    const u16* w1_, const u16* b1_, const u16* w2_, const u16* b2_, const u16* gw_, u16* out_, char* ws_
    ) {
  __shared__ __attribute__((aligned(1024))) char smem[163840];
  LAS char* L = (LAS char*)smem;
  u64 pt0 = 0, pt1 = 0;
  STAMP(0);
  const u32 tid = threadIdx.x, lane = tid & 63, w = __builtin_amdgcn_readfirstlane(tid >> 6);
  const u32 blk = blockIdx.x, xc = blk & 7, ii = blk >> 3;
  v16u ka; v8u kb; v2u kc;
  u64 pc0, tstart; u32 t0_, t1_;
  asm volatile(
      "s_load_dwordx16 %0, %7, 0x0\n\ts_load_dwordx8 %1, %7, 0x40\n\ts_load_dwordx2 %2, %7, 0x60\n\ts_memrealtime %6\n\t"
      "s_getpc_b64 %3\n.Lpf_begin:\n\t"
      "s_min_u32 %4, %8, .Lpf_end-.Lpf_begin\n\ts_load_dword %4, %3, %4\n\t"
      : "=&s"(ka), "=&s"(kb), "=&s"(kc), "=&s"(pc0), "=&s"(t0_), "=&s"(t1_), "=&s"(tstart)
      : "s"(__builtin_amdgcn_kernarg_segment_ptr()), "s"((ii * 8 + w) * 64)
      : "memory", "scc");
  const u32 lr = lane & 15, lg = lane >> 4;
  const u32 ha = 4 * xc + (ii >> 3), jm = ii & 7;
  u32 xo = ((lr >> 3) * 576 + 72 * jm + 18 * lg) * 2, vo = (72 * jm + 18 * lg) * 8192 + ha * 256 + lr * 16;
  PIN(xo); PIN(vo);
  asm volatile("s_waitcnt lgkmcnt(0)" : "+s"(ka), "+s"(kb), "+s"(kc), "+s"(t0_), "+s"(t1_), "+s"(tstart)::"memory");
  const u32 mine = (u32)tstart;
  STAMP(12);
  const GAS u16* x = (const GAS u16*)ptr(ka[0], ka[1]);
  const u64 ckv = ptr(ka[2], ka[3]), wq = ptr(ka[4], ka[5]), wuk = ptr(ka[6], ka[7]), wuv = ptr(ka[8], ka[9]), wo = ptr(ka[10], ka[11]), w1 = ptr(ka[12], ka[13]);
  const GAS u16* b1 = (const GAS u16*)ptr(ka[14], ka[15]);
  const u64 w2 = ptr(kb[0], kb[1]), gw = ptr(kb[4], kb[5]);
  const GAS u16* b2 = (const GAS u16*)ptr(kb[2], kb[3]);
  GAS u16* out = (GAS u16*)ptr(kb[6], kb[7]);
  const u64 ws = ptr(kc[0], kc[1]);
  const __amdgpu_buffer_rsrc_t rws = mk_rsrc(ws, WS_END);
  const u32 epoch = *(const CAS u32*)(ws + WS_E);
  auto get_tag = [&]() { u32 t = epoch; asm volatile("s_add_u32 %0, %0, 1" : "+s"(t)::"scc"); return t; };
  const bool g2role = ii < 16;
  const u32 k = ii & 15;
  const u32 h = 4 * xc + (k >> 2), j = k & 3;
  const u32 g1 = 16 * xc + k, hg = g1 & 1, tile = g1 >> 1, b = tile >> 5;
  const v4u sck = mk_srd(ckv, 2 * 4096 * 1024);
  auto ck_dma = [&](u32 i0, u32 i1) {
#pragma nounroll
    for (u32 i = i0; i < i1; ++i) {
      const u32 row = 16 * w + (i ^ (8 * hg));
      dma(sck, (lane * 16) ^ ((((row & 3) << 2) | ((row >> 2) & 3)) << 4), (tile * 128 + row) * 1024, row * 1024);
    }
  };
  if (w == 0) {
    v4u wqv[18], xa[18];
    v3u xs[3];
    const __amdgpu_buffer_rsrc_t rx = mk_rsrc((u64)x, 2304), rq = mk_rsrc(wq, KNOB(8, 0) & 4 ? 0 : 576 * 8192);
#pragma unroll
    for (int i = 0; i < 3; ++i) xs[i] = __builtin_amdgcn_raw_buffer_load_b96(rx, xo + 12 * i, 0, 0);
#pragma unroll
    for (int s = 0; s < 18; ++s) { wqv[s] = __builtin_amdgcn_raw_buffer_load_b128(rq, vo, s * 8192, NT); SB(); }
    STAMP(13);
    RAWBAR();
    const u32 di = (lr & 7) >> 1, sh = (lr & 1) * 16;
    u32 se[4], so[4];
#pragma unroll
    for (int i = 0; i < 4; ++i) {
      se[i] = di == (u32)i ? ((0x0100u << sh) | (0x0c0c0000u >> sh)) : 0x0c0c0c0cu;
      so[i] = di == (u32)i ? ((0x0302u << sh) | (0x0c0c0000u >> sh)) : 0x0c0c0c0cu;
    }
#pragma unroll
    for (int s = 0; s < 18; ++s)
#pragma unroll
      for (int i = 0; i < 4; ++i) xa[s][i] = __builtin_amdgcn_perm(0u, xs[s / 6][(s % 6) >> 1], (s & 1) ? so[i] : se[i]);
    u32 qo = WS_QP + (ha * 8 + jm) * 1024 + lane * 16;
    PIN(qo);
    const u32 tag = get_tag();
    SB();
    v4f acc = {0.f, 0.f, 0.f, 0.f};
#pragma unroll
    for (int s = 0; s < 18; ++s) acc = mfma(xa[s], wqv[s], acc);
    __builtin_amdgcn_raw_buffer_store_b128(tagged(acc, tag), rws, qo, 0, SC1);
  } else {
    if (w == 7) {
      const v4u sx = mk_srd((u64)x, 2304);
      if (g2role) {
        const v4u sg = mk_srd(gw, 1152);
        dma(sg, lane * 16, 0, L2_GW);
        dma(sx, lane * 16, 0, L2_X); dma(sx, lane * 16, 1024, L2_X + 1024);
        if (lane < 16) dma(sx, lane * 16, 2048, L2_X + 2048);
        if (lane < 8) dma(sg, lane * 16, 1024, L2_GW + 1024);
        if (lane < 4) dma4(mk_srd((u64)b2, 1152), lane * 4, 16 * (9 * xc + k), L2_B2);
      } else {
        dma4(sx, (lane >> 5) * 1152 + (lane & 31) * 4, 72 * k, L1_XR);
        dma4(mk_srd((u64)b1, 9216), lane * 4, 72 * g1, L1_B1);
      }
    }
    if (w == 6 && blk < 8 && lane == 0) __builtin_amdgcn_raw_buffer_store_b32(mine, rws, WS_T0 + blk * 128, 0, SC1);
    STAMP(14);
    RAWBAR();
    STAMP(15);
    if (g2role) {
      SLEEP(0, K_SLEEP_UK);
      const v4u suk = mk_srd(wuk, 512 * 8192);
#pragma nounroll
      for (u32 n = w == 1 ? 0 : 4 * w; n < 4 * w + (KNOB(8, 0) & 8 ? 0 : 4); ++n) {
        const u32 row = 4 * (n & 3) + lg;
        dma(suk, row * 8192 + ((lr ^ row) << 4), (128 * j + 4 * (n & ~3u)) * 8192 + h * 256, L2_UK + n * 1024);
      }
    } else {
      ck_dma(0, KNOB(2, K_EARLY_CK));
      if (w == 1) {
        wait_until(mine + KNOB(1, K_TP));
        u32 e;
        do { ORDER(); e = ld4(rws, WS_T0 + (lane & 7) * 128); } while (!__all(start_ok(e, mine)) && (int)(now() - mine) < 250);
        const u32 t = latest_start(e, mine);
        if (lane == 0) *(LAS u32*)(L + L1_T0) = t;
        wait_until(t + KNOB(9, K_TC));
      }
    }
  }
  STAMP(1);
  const u32 q4 = (lane >> 2) & 3, p4 = lane & 3;
  const u32 fr0 = 4 * (lg & 1) + q4, fr1 = fr0 ^ 4, fpc = 2 * (w & 3) + (p4 >> 1);
  const u32 fa0 = fr0 * 128 + ((fpc ^ (fr0 & 6)) << 4) + 8 * (p4 & 1), fa1 = fr1 * 128 + ((fpc ^ (fr1 & 6)) << 4) + 8 * (p4 & 1);
  const u32 rr = lane >> 3, pcs = ((lane & 7) ^ (rr & 6)) << 4;
  auto frag = [&](u32 a) { return cat(ldstr(L, a + fa0), ldstr(L, a + fa1)); };

  if (g2role) {
    const bool reducer = k < 9;
    const u32 rho = 9 * xc + k, rmt = rho >> 1, rb_ = rho & 1;
    u32 aqp = WS_QP + (h * 8 + w) * 1024 + lane * 16, abq = L2_BQ + ((lg >> 1) * 8 + w) * 272 + (8 * lr + 4 * (lg & 1)) * 2;
    u32 auk = L2_UK + w * 4096 + lr * 256, abr = lr * 272 + 16 * lg, aqg = WS_QG + ((lg >> 1) * 32 + h) * 2048 + (32 * j + 4 * w + (lr >> 2)) * 16;
    u32 auk0 = auk + ((lg ^ lr) << 4), auk1 = auk + (((4 + lg) ^ lr) << 4), auk2 = auk + (((8 + lg) ^ lr) << 4), auk3 = auk + (((12 + lg) ^ lr) << 4);
    PIN(aqp); PIN(abq); PIN(abr); PIN(aqg); PIN(auk0); PIN(auk1); PIN(auk2); PIN(auk3);
    const u32 tag = get_tag();
    wait_until(mine + KNOB(3, K_T1A));
    v4u qp;
    const u32 st0 = ld4(rws, WS_T0 + (lane & 7) * 128);
#pragma nounroll
    for (u32 sp = 0; sp < SPIN_CAP; ++sp) {
      ORDER();
      qp = ld16(rws, aqp);
      COUNT(0);
      if (__all(tagok(qp, tag))) break;
    }
    *(LAS v2u*)(L + abq) = v2u{qp.x, qp.z};
    VMW(0);
    STAMP(2);
    BAR();
    {
      const v4u a0 = lds128(L, auk0), b0 = lds128(L, L2_BQ + abr), a1 = lds128(L, auk1), b1_ = lds128(L, L2_BQ + abr + 64);
      const v4u a2 = lds128(L, auk2), b2_ = lds128(L, L2_BQ + abr + 128), a3 = lds128(L, auk3), b3 = lds128(L, L2_BQ + abr + 192);
      SB();
      v4f acc = {0.f, 0.f, 0.f, 0.f};
      acc = mfma(b0, a0, acc); acc = mfma(b1_, a1, acc); acc = mfma(b2_, a2, acc); acc = mfma(b3, a3, acc);
      const float sc = 0.08838834764831845f * 1.4426950408889634f;
      const float v = add16((acc[0] + acc[1]) + (acc[2] + acc[3])) * sc;
      const u32 p01 = pk(v, dpp<0xB1>(v));
      const u32 p23 = __builtin_amdgcn_update_dpp(0, (int)p01, 0x4E, 0xf, 0xf, true);
      if ((lane & 19) == 0) __builtin_amdgcn_raw_buffer_store_b128(v4u{p01, tag, p23, tag}, rws, aqg, 0, SC1);
    }
    STAMP(3);
    BAR();
    const u32 t0 = latest_start(st0, mine);
    if (PROBE_ID == 20) pt1 = pt0 + 4 * (t0 - mine);
    const v4u suv = mk_srd(wuv, 512 * 8192), swo = mk_srd(wo, 4096 * 1152);
#pragma nounroll
    for (u32 slot = w; slot < 32; slot += 8)
      dma(suv, (16 * (rr & 3) + (rr >> 2)) * 8192 + pcs, (128 * j + 64 * (slot >> 4) + (slot & 14)) * 8192 + h * 256 + (slot & 1) * 128, L2_UV + slot * 1024);
    auto wo_dma = [&](u32 s0, u32 s1, u32 step) {
#pragma nounroll
      for (u32 slot = s0; slot < s1; slot += step) {
        const u32 rbl = (slot * 57) >> 9, cg = slot - 9 * rbl;
        dma(swo, rr * 1152 + pcs, (h * 128 + 8 * ((rbl + 4 * j) & 15)) * 1152 + cg * 128, slot * 1024);
      }
    };
    wo_dma(w, 48, 8);
    VMW(6);
    BAR();
    v4u uvA[4];
#pragma unroll
    for (int s = 0; s < 4; ++s) uvA[s] = frag(L2_UV + ((4 * s + lg) * 2 + (w >> 2)) * 1024);
    float gate0 = 0.f, gate1 = 0.f;
    if (reducer) {
      float a0 = 0.f, a1 = 0.f;
#pragma unroll
      for (int i = 0; i < 5; ++i) {
        const u32 e = lane + 64 * i;
        const u32 x0 = *(LAS u32*)(L + L2_X + e * 4), x1 = *(LAS u32*)(L + L2_X + 1152 + e * 4), gv = *(LAS u32*)(L + L2_GW + e * 4);
        a0 += e < 288 ? bflo(x0) * bflo(gv) + bfhi(x0) * bfhi(gv) : 0.f;
        a1 += e < 288 ? bflo(x1) * bflo(gv) + bfhi(x1) * bfhi(gv) : 0.f;
      }
      gate0 = 1.f / (1.f + __expf(-add32(add16(sum16(a0)))));
      gate1 = 1.f / (1.f + __expf(-add32(add16(sum16(a1)))));
    }
    BAR();
    if (w) wo_dma(47 + w, 144, 7);
    STAMP(4);
    u32 sto = (lane * 2 + (h >> 4)) * 128 + (h & 15) * 8;
    u32 ogo = WS_OG + ((h * 4 + j) * 64 + 8 * w + lg) * 512 + lr * 16;
    u32 awo = lg * 9216 + (w >> 2) * 1024, af = L2_F + (8 * w + lg) * 4;
    const u32 U1 = 16 * (lg & 1) + lr, U2 = 4 * w + (lr >> 2);
    u32 abc = L2_BC + ((w >> 2) * 8 + (w & 3) * 2 + (lg >> 1)) * 272 + (U1 ^ ((U1 >> 1) & 1)) * 8;
    u32 act = L2_CT + (lg >> 1) * 272 + (U2 ^ ((U2 >> 1) & 1)) * 8 + (lr & 3) * 2, ail = L2_IL + (lg >> 1) * 4;
    u32 acr[4];
#pragma unroll
    for (int s = 0; s < 4; ++s) { acr[s] = L2_CT + (lr & 1) * 272 + (32 * ((s + j) & 3) + 8 * lg) * 2; PIN(acr[s]); }
    u32 fo = WS_FG + (128 + h * 4 + j) * 128 + lr * 8 + (w + 8 * lg) * 32768, fro = WS_FG + rmt * 32768 + (tid >> 2) * 128 + rb_ * 64 + (tid & 3) * 16;
    u32 sel[4];
    {
      const u32 di = (lr & 7) >> 1, sh = (lr & 1) * 16, src = (lr >> 3) ? 0x0302u : 0x0100u;
#pragma unroll
      for (int i = 0; i < 4; ++i) { sel[i] = di == (u32)i ? ((src << sh) | (0x0c0c0000u >> sh)) : 0x0c0c0c0cu; PIN(sel[i]); }
    }
    PIN(sto); PIN(ogo); PIN(awo); PIN(af); PIN(abc); PIN(act); PIN(ail); PIN(fo); PIN(fro); PIN(gate0); PIN(gate1);
    STAMP(5);
    float fch = 0.f;
    if (w == 0) {
      wait_until(t0 + KNOB(5, K_TM));
      v2u mv;
#pragma nounroll
      for (u32 sp = 0; sp < SPIN_CAP; ++sp) {
        ORDER();
        mv = ld8(rws, WS_ST + sto);
        COUNT(1);
        if (__all(mv.y == tag)) break;
      }
      float M = f_(mv.x);
      M = fmaxf(M, dpp<0xB1>(M)); M = fmaxf(M, dpp<0x4E>(M)); M = fmaxf(M, dpp<0x141>(M)); M = fmaxf(M, dpp<0x140>(M)); M = max16(M);
      fch = __builtin_amdgcn_exp2f(f_(mv.x) - M);
      const u32 hi = pk(fch, 0.f) & 0xffffu;
      *(LAS u32*)(L + L2_F + lane * 4) = pk(fch, fch - bflo(hi));
    }
    BAR();
    STAMP(6);
    v4u og[4];
    v2u lv = {0u, tag};
#pragma unroll
    for (int e = 0; e < 4; ++e) og[e] = ld16(rws, ogo + (e >> 1) * 2048 + (e & 1) * 256);
    if (w == 0) lv = ld8(rws, WS_SL + sto);
    ORDER();
    v4u woA[5][4], fA[2];
#pragma unroll
    for (int e = 0; e < 2; ++e) {
      const u32 fv = *(LAS u32*)(L + af + 16 * e);
#pragma unroll
      for (int i = 0; i < 4; ++i) fA[e][i] = __builtin_amdgcn_perm(0u, fv, sel[i]);
    }
    ORDER();
#pragma nounroll
    for (u32 sp = 0; sp < SPIN_CAP; ++sp) {
      const bool k0 = __all(tagok(og[0], tag) & tagok(og[1], tag)), k1 = __all(tagok(og[2], tag) & tagok(og[3], tag) & (lv.y == tag));
      if (k0 & k1) break;
      COUNT(2);
      ORDER();
      if (!k0) { og[0] = ld16(rws, ogo); og[1] = ld16(rws, ogo + 256); }
      if (!k1) { og[2] = ld16(rws, ogo + 2048); og[3] = ld16(rws, ogo + 2304); if (w == 0) lv = ld8(rws, WS_SL + sto); }
    }
    VMW(0);
    STAMP(7);
    {
      v4f acc = {0.f, 0.f, 0.f, 0.f};
      acc = mfma(fA[0], v4u{og[0].x, og[0].z, og[1].x, og[1].z}, acc);
      acc = mfma(fA[1], v4u{og[2].x, og[2].z, og[3].x, og[3].z}, acc);
      *(LAS v2u*)(L + abc) = v2u{pk(acc[0], acc[1]), pk(acc[2], acc[3])};
      if (w == 0) {
        const float ls = add16(sum16(fch * f_(lv.x)));
        if ((lane & 31) == 0) *(LAS float*)(L + L2_IL + (lane >> 5) * 4) = 1.f / ls;
      }
    }
    BAR();
    {
      const v4u c0 = lds128(L, L2_BC + abr), c1 = lds128(L, L2_BC + abr + 64), c2 = lds128(L, L2_BC + abr + 128), c3 = lds128(L, L2_BC + abr + 192);
      const float il = *(LAS float*)(L + ail);
      SB();
#pragma unroll
    for (int n = 0; n < 4; ++n)
#pragma unroll
      for (int s = 0; s < 4; ++s) woA[n][s] = frag(awo + s * 36864 + n * 2048);
#pragma unroll
    for (int s = 0; s < 4; ++s) woA[4][s] = frag(lg * 9216 + s * 36864 + 8192);
      SB();
      v4f acc = {0.f, 0.f, 0.f, 0.f};
      acc = mfma(c0, uvA[0], acc); acc = mfma(c1, uvA[1], acc); acc = mfma(c2, uvA[2], acc); acc = mfma(c3, uvA[3], acc);
      const float v = add16((acc[0] + acc[1]) + (acc[2] + acc[3])) * il;
      if (!(lg & 1)) *(LAS u16*)(L + act) = (u16)pk(v, v);
    }
    BAR();
    STAMP(8);
    {
      v4u cb[4];
#pragma unroll
      for (int s = 0; s < 4; ++s) cb[s] = lds128(L, acr[s]);
      SB();
      v4f acc[5];
#pragma unroll
      for (int n = 0; n < 5; ++n) acc[n] = v4f{0.f, 0.f, 0.f, 0.f};
#pragma unroll
      for (int n = 0; n < 4; ++n)
#pragma unroll
        for (int s = 0; s < 4; ++s) acc[n] = mfma(cb[s], woA[n][s], acc[n]);
      __builtin_amdgcn_raw_buffer_store_b64(v2u{sel4(acc[0], acc[1], acc[2], acc[3], lg), tag}, rws, fo, 0, SC1);
      if (w < 4) {
#pragma unroll
        for (int s = 0; s < 4; ++s) acc[4] = mfma(cb[s], woA[4][s], acc[4]);
        if (lane < 16) __builtin_amdgcn_raw_buffer_store_b64(v2u{pk(acc[4][0], acc[4][1]), tag}, rws, fo, 32 * 32768, SC1);
      }
    }
    STAMP(9);
    if (reducer) {
      wait_until(t0 + KNOB(7, K_TF));
      v4u pm = ld16(rws, fro), pa = ld16(rws, fro + 16384);
#pragma nounroll
      for (u32 sp = 0; sp < SPIN_CAP; ++sp) {
        const bool k0 = __all(tagok(pm, tag)), k1 = __all(tagok(pa, tag));
        if (k0 & k1) break;
        COUNT(3);
        ORDER();
        if (!k0) pm = ld16(rws, fro);
        if (!k1) pa = ld16(rws, fro + 16384);
      }
      STAMP(10);
      v4f v = {gate0 * bflo(pa.x) + (1.f - gate0) * bflo(pm.x), gate1 * bfhi(pa.x) + (1.f - gate1) * bfhi(pm.x),
               gate0 * bflo(pa.z) + (1.f - gate0) * bflo(pm.z), gate1 * bfhi(pa.z) + (1.f - gate1) * bfhi(pm.z)};
#pragma unroll
      for (int i = 0; i < 4; ++i) {
        float t = v[i];
        t += dpp<0x124>(t); t += dpp<0x128>(t);
        v[i] = add32(add16(t));
      }
      if (lane < 4) *(LAS v4f*)(L + L2_RD + w * 64 + lane * 16) = v;
      BAR();
      if (tid < 16) {
        float s = (1.f - (tid & 1 ? gate1 : gate0)) * bflo(*(LAS u16*)(L + L2_B2 + (tid >> 1) * 2));
#pragma unroll
        for (int i = 0; i < 8; ++i) s += *(LAS float*)(L + L2_RD + i * 64 + tid * 4);
        out[(tid & 1) * 576 + 8 * rho + (tid >> 1)] = (u16)pk(s, 0.f);
      }
      if (blk == 0 && tid == 0) __hip_atomic_store((GAS u32*)(ws + WS_E), tag, __ATOMIC_RELAXED, __HIP_MEMORY_SCOPE_AGENT);
      STAMP(11);
    }
  } else {
    const u32 hu = tid >> 4, hf = 2 * hu + ((lr >> 1) & 1);
    BAR();
    ck_dma(w == 0 ? 0 : KNOB(2, K_EARLY_CK), 16);
    STAMP(2);
    const u32 t0 = __builtin_amdgcn_readfirstlane(*(LAS u32*)(L + L1_T0));
    const u32 sg = (0x9cu >> (2 * lg)) & 3;
    const u32 pir = ((lr & 3) << 2) | (lr >> 2);
    u32 qo = WS_QG + (b * 32 + hg * 16) * 2048 + tid * 16;
    u32 akr[4], aqr[4], aqw[4];
#pragma unroll
    for (int s = 0; s < 4; ++s) {
      akr[s] = (16 * w + lr) * 1024 + (((4 * s + sg) ^ pir) << 4);
      aqr[s] = L1_Q + lr * 1024 + (((4 * s + sg) ^ lr) << 4);
      const u32 head = 4 * s + (w >> 1), c4 = tid & 127;
      aqw[s] = L1_Q + head * 1024 + (((c4 >> 1) ^ head) << 4) + (c4 & 1) * 8;
      PIN(akr[s]); PIN(aqr[s]); PIN(aqw[s]);
    }
    u32 amx = L1_MX + lr * 32, apw = L1_P + lr * 272 + (16 * w + 4 * lg) * 2, apr = L1_P + lr * 272 + 16 * lg;
    u32 oo = WS_OG + (((hg * 16 + 4 * lg) * 4 + (w >> 1)) * 64 + tile) * 512 + (w & 1) * 256 + lr * 16, sto = g1 * 128 + lr * 8;
    PIN(qo); PIN(amx); PIN(apw); PIN(apr); PIN(oo); PIN(sto);
    VMW(0);
    const u32 tag = get_tag();
    STAMP(3);
    v4u kA[16];
#pragma unroll
    for (int s = 0; s < 16; ++s) kA[s] = lds128(L, akr[s & 3] + (s >> 2) * 256);
    wait_until(t0 + KNOB(10, K_TQ));
    v4u qg[4];
#pragma unroll
    for (int u = 0; u < 4; ++u) qg[u] = ld16(rws, qo + u * 8192);
    ORDER();
    BAR();
    if (tid < 128) {
      const u32 U = lane >> 2, xv = *(LAS u16*)(L + L1_XR + w * 128 + lane * 2);
      *(LAS u16*)(L + L1_XB + w * 128 + (U ^ ((U >> 1) & 1)) * 8 + (lane & 3) * 2) = lane < 36 ? (u16)xv : (u16)0;
      *(LAS u16*)(L + L1_HB + tid * 2) = 0;
    }
    v4u vA[4][4];
#pragma unroll
    for (int tt = 0; tt < 4; ++tt) {
      const u32 a0 = (8 * lg + q4) * 1024 + (((8 * w + 2 * tt + (p4 >> 1)) ^ ((q4 << 2) | (2 * (lg & 1)))) << 4) + 8 * (p4 & 1);
      const u32 a1 = (a0 ^ 16) + 4096;
#pragma unroll
      for (int t = 0; t < 4; ++t) vA[tt][t] = cat(ldstr(L, a0 + t * 32768), ldstr(L, a1 + t * 32768));
    }
    const v4u sw1 = mk_srd(w1, 576 * 9216), sw2 = mk_srd(w2, 4608 * 1152);
    u32 w1v = rr * 9216 + pcs, w1s[4];
#pragma unroll
    for (int n = 0; n < 4; ++n) {
      const u32 slot = w + 8 * n, rbl = (slot * 57) >> 9;
      w1s[n] = (36 * k + 8 * rbl) * 9216 + xc * 1152 + (slot - 9 * rbl) * 128;
      asm volatile("" : "+s"(w1s[n]));
    }
    PIN(w1v);
    ORDER();
#pragma nounroll
    for (u32 sp = 0; sp < SPIN_CAP; ++sp) {
      const bool k0 = __all(tagok(qg[0], tag)), k1 = __all(tagok(qg[1], tag)), k2 = __all(tagok(qg[2], tag)), k3 = __all(tagok(qg[3], tag));
      if (k0 & k1 & k2 & k3) break;
      COUNT(0);
      ORDER();
      if (!k0) qg[0] = ld16(rws, qo);
      if (!k1) qg[1] = ld16(rws, qo + 8192);
      if (!k2) qg[2] = ld16(rws, qo + 16384);
      if (!k3) qg[3] = ld16(rws, qo + 24576);
    }
#pragma unroll
    for (int u = 0; u < 4; ++u) *(LAS v2u*)(L + aqw[u]) = v2u{qg[u].x, qg[u].z};
    STAMP(4);
    BAR();
    auto w_dma1 = [&](v4u srd, u32 pitch, u32 base, u32 lds, u32 slot) {
      const u32 rbl = (slot * 57) >> 9, cg = slot - 9 * rbl;
      dma(srd, (slot >= 36 && rr >= 4 ? 0x80000000u : rr * pitch) + pcs, base + 8 * rbl * pitch + cg * 128, lds + slot * 1024);
    };
    auto w_dma = [&](v4u srd, u32 pitch, u32 base, u32 lds, u32 first) {
#pragma nounroll
      for (u32 slot = first + w; slot < 45; slot += 8) w_dma1(srd, pitch, base, lds, slot);
    };
    v4f S = {0.f, 0.f, 0.f, 0.f};
    {
      v4u qB[16];
#pragma unroll
      for (int s = 0; s < 16; ++s) qB[s] = lds128(L, aqr[s & 3] + (s >> 2) * 256);
      SB();
#pragma unroll
      for (int s = 0; s < 16; ++s) {
        S = mfma(kA[s], qB[s], S);
        if ((s & 3) == 1) { dma(sw1, w1v, w1s[s >> 2], L1_W1 + (w + 2 * (s & 12)) * 1024); SB(); }
      }
    }
    {
      const float mw = max32(max16(fmaxf(fmaxf(S[0], S[1]), fmaxf(S[2], S[3]))));
      if (lane < 16) *(LAS float*)(L + amx + w * 4) = mw;
    }
    BAR();
    STAMP(5);
    {
      const v4f m0 = *(LAS v4f*)(L + amx), m1 = *(LAS v4f*)(L + amx + 16);
      const float M = fmaxf(fmaxf(fmaxf(m0[0], m0[1]), fmaxf(m0[2], m0[3])), fmaxf(fmaxf(m1[0], m1[1]), fmaxf(m1[2], m1[3])));
      const float p0 = __builtin_amdgcn_exp2f(S[0] - M), p1 = __builtin_amdgcn_exp2f(S[1] - M), p2 = __builtin_amdgcn_exp2f(S[2] - M), p3 = __builtin_amdgcn_exp2f(S[3] - M);
      *(LAS v2u*)(L + apw) = v2u{pk(p0, p1), pk(p2, p3)};
      if (tid < 16) __builtin_amdgcn_raw_buffer_store_b64(v2u{u_(M), tag}, rws, sto, WS_ST, SC1);
    }
    BAR();
    {
      v4u pB[4];
#pragma unroll
      for (int t = 0; t < 4; ++t) pB[t] = lds128(L, apr + 64 * t);
      SB();
      v4f o[4];
#pragma unroll
      for (int tt = 0; tt < 4; ++tt) o[tt] = v4f{0.f, 0.f, 0.f, 0.f};
#pragma unroll
      for (int t = 0; t < 4; ++t)
#pragma unroll
        for (int tt = 0; tt < 4; ++tt) o[tt] = mfma(pB[t], vA[tt][t], o[tt]);
#pragma unroll
      for (int r = 0; r < 4; ++r)
        __builtin_amdgcn_raw_buffer_store_b128(v4u{pk(o[0][r], o[1][r]), tag, pk(o[2][r], o[3][r]), tag}, rws, oo, r * 4 * 64 * 512, SC1);
      if (w == 7) {
        v4f acc = {0.f, 0.f, 0.f, 0.f};
        const v4u ones = {0x3f803f80u, 0x3f803f80u, 0x3f803f80u, 0x3f803f80u};
#pragma unroll
        for (int t = 0; t < 4; ++t) acc = mfma(ones, pB[t], acc);
        if (lane < 16) __builtin_amdgcn_raw_buffer_store_b64(v2u{u_(acc[0]), tag}, rws, sto, WS_SL, SC1);
      }
    }
    STAMP(6);
    w_dma(sw1, 9216, 36 * k * 9216 + xc * 1152, L1_W1, K_W1_EARLY ? 32 : 0);
    w_dma(sw2, 1152, 36 * g1 * 1152, L1_W2, 0);
    auto gemv36 = [&](u32 lw, u32 lb, u32 vo, u32 stride) {
      const v4u h0 = lds128(L, lb + (lr & 1) * 128 + 16 * lg), h1 = lds128(L, lb + (lr & 1) * 128 + 64 + 16 * lg);
      v4u a0[5], a1[5];
#pragma unroll
      for (int n = 0; n < 5; ++n) {
        const u32 cg = n < 4 ? 2 * n + (w >> 2) : 8;
        a0[n] = frag(lw + (lg * 9 + cg) * 1024);
        a1[n] = frag(lw + (36 + cg) * 1024);
      }
      SB();
      v4f acc[5];
#pragma unroll
      for (int n = 0; n < 5; ++n) acc[n] = mfma(h1, a1[n], mfma(h0, a0[n], v4f{0.f, 0.f, 0.f, 0.f}));
      __builtin_amdgcn_raw_buffer_store_b64(v2u{sel4(acc[0], acc[1], acc[2], acc[3], lg), tag}, rws, vo, 0, SC1);
      if (w < 4 && lane < 16) __builtin_amdgcn_raw_buffer_store_b64(v2u{pk(acc[4][0], acc[4][1]), tag}, rws, vo, 32 * stride, SC1);
    };
    if (w < 5) { VMW(6); } else { VMW(5); }
    BAR();
    STAMP(7);
    gemv36(L1_W1, L1_XB, WS_HG + g1 * 4608 + (w + 8 * lg) * 128 + lr * 8, 128);
    STAMP(8);
    wait_until(t0 + KNOB(6, K_TH));
    if (w < 5) {
      const u32 ho = WS_HG + (16 * xc + lr) * 4608 + (18 * k + min(hu, 17u)) * 16;
      v4u hp;
#pragma nounroll
      for (u32 sp = 0; sp < SPIN_CAP; ++sp) {
        ORDER();
        hp = ld16(rws, ho);
        COUNT(1);
        if (__all(tagok(hp, tag))) break;
      }
      const float s0 = sum16(bflo(hp.x)), s1 = sum16(bfhi(hp.x)), s2 = sum16(bflo(hp.z)), s3 = sum16(bfhi(hp.z));
      const float v = (lr == 0 ? s0 : lr == 1 ? s1 : lr == 2 ? s2 : s3) + bflo(*(LAS u16*)(L + L1_B1 + hf * 2));
      const float u2 = 1.5957691216057308f * (v + 0.044715f * v * v * v);
      const float hv = v / (1.f + __expf(-u2));
      if (tid < 288 && lr < 4) *(LAS u16*)(L + L1_HB + (lr & 1) * 128 + ((hf >> 2) ^ ((hf >> 3) & 1)) * 8 + (hf & 3) * 2) = (u16)pk(hv, 0.f);
    }
    VMW(0);
    STAMP(9);
    BAR();
    gemv36(L1_W2, L1_HB, WS_FG + g1 * 128 + lr * 8 + (w + 8 * lg) * 32768, 32768);
    STAMP(10);
  }
  STAMP_OUT();
  asm volatile(".Lpf_end:");
}

extern "C" void kernel_launch(void* const* d_in, const int* in_sizes, int n_in, void* d_out, int out_size, void* d_ws, size_t ws_size, hipStream_t stream) {
  fused_decode<<<256, 512, 0, stream>>>((const u16*)d_in[0], (const u16*)d_in[1], (const u16*)d_in[2], (const u16*)d_in[3], (const u16*)d_in[4],
      (const u16*)d_in[5], (const u16*)d_in[7], (const u16*)d_in[8], (const u16*)d_in[9], (const u16*)d_in[10], (const u16*)d_in[11], (u16*)d_out, (char*)d_ws + WS_BASE(ws_size)
      );
}
```

```cpp
#include <hip/hip_runtime.h>
#include <stdint.h>

typedef unsigned u32;
typedef unsigned long long u64;
typedef unsigned short u16;
typedef u32 v4u __attribute__((ext_vector_type(4)));
typedef u32 v3u __attribute__((ext_vector_type(3)));
typedef u32 v2u __attribute__((ext_vector_type(2)));
typedef float v4f __attribute__((ext_vector_type(4)));
typedef float v2f __attribute__((ext_vector_type(2)));
typedef short v8s __attribute__((ext_vector_type(8)));
typedef short v4s __attribute__((ext_vector_type(4)));
typedef __bf16 v2b __attribute__((ext_vector_type(2)));
#define LAS __attribute__((address_space(3)))
#define GAS __attribute__((address_space(1)))
#define CAS __attribute__((address_space(4)))
#define DI __device__ __forceinline__

#define K_PREFETCH 1
#define K_T1A 250
#define K_TUK 60
#define K_TS3 300
#define K_EARLY 3
#define K_W2N 3
#define K_TMAP 0
#define K_W1N 0
#define K_NT_CK 0
#define K_NT_W 1
#define K_NT_Q 1
#define K_TP 90
#define K_TC 820
#define K_TQ 370
#define K_TM 660
#define K_TH 700
#define K_TF 930
#define K_WS_END 0
#define WS_BASE(sz) (K_WS_END ? (((sz) - (16u << 20)) & ~(size_t)4095) : (size_t)0)
constexpr u32 WS_E = 0;
constexpr u32 WS_T0 = 1024;
constexpr u32 WS_QP = 4096;
constexpr u32 WS_QG = WS_QP + 32 * 8 * 1024;
constexpr u32 WS_ML = WS_QG + 2 * 32 * 2048;
constexpr u32 WS_CK = WS_ML + 128 * 256;
constexpr u32 WS_CG = WS_CK + 128 * 512;
constexpr u32 WS_HG = WS_CG + 128 * 1024;
constexpr u32 WS_FG = WS_HG + 128 * 2 * 2304;
constexpr u32 WS_OG = WS_FG + 36 * 128 * 128;
constexpr u32 OG_H = 4 * 64 * 256;
constexpr u32 WS_END = WS_OG + 32 * OG_H;
constexpr u32 WS_DBG = 8u << 20;
constexpr u32 L1_Q = 131072, L1_MX = L1_Q + 16384, L1_P = L1_MX + 512, L1_HB = L1_P + 4352, L1_B1 = L1_HB + 384, L1_T0 = L1_B1 + 256, L1_GT = L1_T0 + 8, L1_CW = L1_T0 + 16, L1_X = L1_CW + 8 * 640, L1_GW = L1_X + 2304, L1_END = L1_GW + 1280;
constexpr u32 L1_W2 = 0, L1_WO = 46080;
constexpr u32 L2_W1 = 0, L2_UV = 49152, L2_UK = 81920;
constexpr u32 L2_BQ = 131072, L2_BC = L2_BQ + 4352, L2_F = L2_BC + 4352, L2_IL = L2_F + 256, L2_RD = L2_IL + 32, L2_X = L2_RD + 512, L2_GW = L2_X + 2304, L2_B2 = L2_GW + 1280, L2_XB = L2_B2 + 16;
static_assert(L1_END <= 162816 && L1_CW % 16 == 0 && L1_X % 16 == 0 && L2_XB + 256 <= 162816, "lds");
static_assert(L1_HB % 16 == 0 && L2_XB % 16 == 0 && L2_X % 16 == 0 && L2_GW % 16 == 0 && L2_RD % 16 == 0, "align");
constexpr int SC1 = 16, NT = 2;
constexpr u32 SPIN_CAP = 1u << 15;

#define SLEEP(idx, dflt) do { if (dflt) __builtin_amdgcn_s_sleep(dflt); } while (0)
#define KNOB(idx, dflt) (dflt)
#define STAMP(i) do {} while (0)
#define COUNT(i) do {} while (0)
#define STAMP_OUT() do {} while (0)

DI v4u mk_srd(u64 a, u32 bytes) {
  return v4u{(u32)a, (u32)(a >> 32) & 0xffffu, bytes, 0x00020000u};
}
DI __amdgpu_buffer_rsrc_t mk_rsrc(u64 p, u32 bytes) { return __builtin_amdgcn_make_buffer_rsrc((void*)p, 0, (int)bytes, 0x00020000); }
DI void dman(v4u srd, u32 voff, u32 soff, u32 lds) {
  asm volatile("s_mov_b32 m0, %3\n\ts_nop 0\n\tbuffer_load_dwordx4 %0, %1, %2 offen nt lds" ::"v"(voff), "s"(srd), "s"(soff), "s"(lds) : "memory");
}
DI void dmac(v4u srd, u32 voff, u32 soff, u32 lds);
DI void dma(v4u srd, u32 voff, u32 soff, u32 lds) { if (K_NT_W) dman(srd, voff, soff, lds); else dmac(srd, voff, soff, lds); }
DI void dmac(v4u srd, u32 voff, u32 soff, u32 lds) {
  asm volatile("s_mov_b32 m0, %3\n\ts_nop 0\n\tbuffer_load_dwordx4 %0, %1, %2 offen lds" ::"v"(voff), "s"(srd), "s"(soff), "s"(lds) : "memory");
}
DI void dma4(v4u srd, u32 voff, u32 soff, u32 lds) {
  asm volatile("s_mov_b32 m0, %3\n\ts_nop 0\n\tbuffer_load_dword %0, %1, %2 offen lds" ::"v"(voff), "s"(srd), "s"(soff), "s"(lds) : "memory");
}
#define BAR() asm volatile("s_waitcnt lgkmcnt(0)\n\ts_barrier" ::: "memory")
#define RAWBAR() asm volatile("s_barrier" ::: "memory")
#define VMW(n) asm volatile("s_waitcnt vmcnt(" #n ")" ::: "memory")
#define STR2(x) #x
#define STR(x) STR2(x)
#define ORDER() asm volatile("" ::: "memory")
#define PIN(x) asm volatile("" : "+v"(x))
#define SB() __builtin_amdgcn_sched_barrier(0)
DI v4f mfma(v4u a, v4u b, v4f c) { return __builtin_amdgcn_mfma_f32_16x16x32_bf16(__builtin_bit_cast(v8s, a), __builtin_bit_cast(v8s, b), c, 0, 0, 0); }
DI u32 pk(float a, float b) { return __builtin_bit_cast(u32, __builtin_convertvector(v2f{a, b}, v2b)); }
DI float bflo(u32 x) { return __builtin_bit_cast(float, x << 16); }
DI float bfhi(u32 x) { return __builtin_bit_cast(float, x & 0xffff0000u); }
DI float f_(u32 x) { return __builtin_bit_cast(float, x); }
DI u32 u_(float x) { return __builtin_bit_cast(u32, x); }
template <int C> DI float dpp(float x) { return f_((u32)__builtin_amdgcn_update_dpp(0, (int)u_(x), C, 0xf, 0xf, true)); }
DI float sum8(float x) { x += dpp<0xB1>(x); x += dpp<0x4E>(x); x += dpp<0x141>(x); return x; }
DI float sum16(float x) { x = sum8(x); x += dpp<0x140>(x); return x; }
DI float add16(float x) { auto r = __builtin_amdgcn_permlane16_swap(u_(x), u_(x), false, false); return f_(r[0]) + f_(r[1]); }
DI float add32(float x) { auto r = __builtin_amdgcn_permlane32_swap(u_(x), u_(x), false, false); return f_(r[0]) + f_(r[1]); }
DI float max16(float x) { auto r = __builtin_amdgcn_permlane16_swap(u_(x), u_(x), false, false); return fmaxf(f_(r[0]), f_(r[1])); }
DI float max32(float x) { auto r = __builtin_amdgcn_permlane32_swap(u_(x), u_(x), false, false); return fmaxf(f_(r[0]), f_(r[1])); }
DI v4u lds128(LAS char* L, u32 a) { return *(LAS v4u*)(L + a); }
DI v2u ldstr(LAS char* L, u32 a) { return __builtin_bit_cast(v2u, __builtin_amdgcn_ds_read_tr16_b64_v4i16((LAS v4s*)(L + a))); }
DI v4u cat(v2u a, v2u b) { return v4u{a.x, a.y, b.x, b.y}; }
DI u32 bad(v4u a, u32 tag) { return (a.y ^ tag) | (a.w ^ tag); }
DI bool allok(u32 d) { return __builtin_amdgcn_ballot_w64(d != 0) == 0; }
DI v4u tagged(v4f a, u32 tag) { return v4u{pk(a[0], a[1]), tag, pk(a[2], a[3]), tag}; }
DI u32 sel4(v4f a0, v4f a1, v4f a2, v4f a3, u32 g) {
  u32 p0 = pk(a0[0], a0[1]), p1 = pk(a1[0], a1[1]), p2 = pk(a2[0], a2[1]), p3 = pk(a3[0], a3[1]);
  p0 = g & 1 ? p1 : p0;
  p2 = g & 1 ? p3 : p2;
  return g & 2 ? p2 : p0;
}
typedef unsigned short v2h __attribute__((ext_vector_type(2)));
DI u32 coef(u32 i) { return ((i * 0x9e3779b1u + 0x7f4a7c15u) ^ ((i * 0x85ebca6bu) >> 13)) | 0x00010001u; }
DI u32 hdot(u32 d, u32 c, u32 acc) { return __builtin_amdgcn_udot2(__builtin_bit_cast(v2h, d), __builtin_bit_cast(v2h, c), acc, false); }
template <int C> DI u32 dppu(u32 x) { return (u32)__builtin_amdgcn_update_dpp(0, (int)x, C, 0xf, 0xf, true); }
DI u32 usum8(u32 x) { x += dppu<0xB1>(x); x += dppu<0x4E>(x); x += dppu<0x141>(x); return x; }
DI u32 now() { u64 t; asm volatile("s_memrealtime %0\n\ts_waitcnt lgkmcnt(0)" : "=s"(t)::"memory"); return (u32)t; }
DI void wait_until(u32 t) { while ((int)(now() - t) < 0) __builtin_amdgcn_s_sleep(1); }
DI bool start_ok(u32 e, u32 mine) { return e - mine + 300u <= 600u; }
DI u32 latest_start(u32 e, u32 mine) {
  int d = start_ok(e, mine) ? (int)(e - mine) : 0;
  d = max(d, __builtin_amdgcn_update_dpp(0, d, 0xB1, 0xf, 0xf, true));
  d = max(d, __builtin_amdgcn_update_dpp(0, d, 0x4E, 0xf, 0xf, true));
  d = max(d, __builtin_amdgcn_update_dpp(0, d, 0x141, 0xf, 0xf, true));
  return mine + (u32)__builtin_amdgcn_readfirstlane(d);
}
DI u32 ld4(__amdgpu_buffer_rsrc_t r, u32 o) { return __builtin_amdgcn_raw_buffer_load_b32(r, o, 0, SC1); }
DI v4u ld16(__amdgpu_buffer_rsrc_t r, u32 o) { return __builtin_amdgcn_raw_buffer_load_b128(r, o, 0, SC1); }
DI v2u ld8(__amdgpu_buffer_rsrc_t r, u32 o) { return __builtin_amdgcn_raw_buffer_load_b64(r, o, 0, SC1); }

typedef u32 v16u __attribute__((ext_vector_type(16)));
typedef u32 v8u __attribute__((ext_vector_type(8)));
DI u64 ptr(u32 lo, u32 hi) { return (u64)lo | ((u64)hi << 32); }

__global__ void __launch_bounds__(512, 2) fused_decode(const u16* x_, const u16* ckv_, const u16* wq_, const u16* wuk_, const u16* wuv_, const u16* wo_,
    const u16* w1_, const u16* b1_, const u16* w2_, const u16* b2_, const u16* gw_, u16* out_, char* ws_
    ) {
  __shared__ __attribute__((aligned(1024))) char smem[163840];
  LAS char* L = (LAS char*)smem;
  STAMP(0);
  const u32 tid = threadIdx.x, lane = tid & 63, w = __builtin_amdgcn_readfirstlane(tid >> 6);
  const u32 blk = blockIdx.x, xc = blk & 7, ii = blk >> 3;
  v16u ka; v8u kb; v2u kc;
  u64 pc0, tstart; u32 t0_, t1_;
  asm volatile(
      "s_load_dwordx16 %0, %7, 0x0\n\ts_load_dwordx8 %1, %7, 0x40\n\ts_load_dwordx2 %2, %7, 0x60\n\ts_memrealtime %6\n\t"
      "s_getpc_b64 %3\n.Lpf_begin:\n\t"
      "s_min_u32 %4, %8, .Lpf_end-.Lpf_begin\n\ts_load_dword %4, %3, %4\n\t"
      : "=&s"(ka), "=&s"(kb), "=&s"(kc), "=&s"(pc0), "=&s"(t0_), "=&s"(t1_), "=&s"(tstart)
      : "s"(__builtin_amdgcn_kernarg_segment_ptr()), "s"((ii * 8 + w) * 64)
      : "memory", "scc");
  const u32 lr = lane & 15, lg = lane >> 4;
  const u32 ha = 4 * xc + (ii >> 3), jm = ii & 7;
  u32 xo = ((lr >> 3) * 576 + 72 * jm + 18 * lg) * 2, vo = (72 * jm + 18 * lg) * 8192 + ha * 256 + lr * 16;
  u32 ukv = (4 * (w & 3) + lg) * 8192 + ((lr ^ (4 * (w & 3) + lg)) << 4);
  PIN(xo); PIN(vo); PIN(ukv);
  asm volatile("s_waitcnt lgkmcnt(0)" : "+s"(ka), "+s"(kb), "+s"(kc), "+s"(t0_), "+s"(t1_), "+s"(tstart)::"memory");
  const u32 mine = (u32)tstart;
  const GAS u16* x = (const GAS u16*)ptr(ka[0], ka[1]);
  const u64 ckv = ptr(ka[2], ka[3]), wq = ptr(ka[4], ka[5]), wuk = ptr(ka[6], ka[7]), wuv = ptr(ka[8], ka[9]), wo = ptr(ka[10], ka[11]), w1 = ptr(ka[12], ka[13]);
  const GAS u16* b1 = (const GAS u16*)ptr(ka[14], ka[15]);
  const u64 w2 = ptr(kb[0], kb[1]), gw = ptr(kb[4], kb[5]);
  const GAS u16* b2 = (const GAS u16*)ptr(kb[2], kb[3]);
  GAS u16* out = (GAS u16*)ptr(kb[6], kb[7]);
  const u64 ws = ptr(kc[0], kc[1]);
  const __amdgpu_buffer_rsrc_t rws = mk_rsrc(ws, WS_END);
  const u32 epoch = *(const CAS u32*)(ws + WS_E);
  auto get_tag = [&]() { u32 t = epoch; asm volatile("s_add_u32 %0, %0, 1" : "+s"(t)::"scc"); return t; };
  const bool g2role = ii < 16;
  const u32 k = ii & 15;
  const u32 h = 4 * xc + (k >> 2), j = k & 3;
  const u32 g1 = 16 * xc + k, hg = g1 & 1, tile = KNOB(6, K_TMAP) ? (k >> 1) * 8 + xc : g1 >> 1, b = tile >> 5, pidx = 2 * tile + hg;
  const v4u sck = mk_srd(ckv, 2 * 4096 * 1024);
  auto ck_dma = [&](u32 i0, u32 i1) {
#pragma nounroll
    for (u32 i = i0; i < i1; ++i) {
      const u32 row = 16 * w + 2 * (i & 7) + ((i >> 3) ^ hg);
      (K_NT_CK ? dman : dmac)(sck, (lane * 16) ^ ((((row & 3) << 2) | ((row >> 2) & 3)) << 4), (tile * 128 + row) * 1024, row * 1024);
    }
  };
  if (w == 0) {
    __builtin_amdgcn_s_setprio(3);
    v4u wqv[18], xa[18];
    v3u xs[3];
    const __amdgpu_buffer_rsrc_t rx = mk_rsrc((u64)x, 2304), rq = mk_rsrc(wq, 576 * 8192);
#pragma unroll
    for (int i = 0; i < 3; ++i) xs[i] = __builtin_amdgcn_raw_buffer_load_b96(rx, xo + 12 * i, 0, 0);
#pragma unroll
    for (int s = 0; s < 18; ++s) { wqv[s] = __builtin_amdgcn_raw_buffer_load_b128(rq, vo, s * 8192, K_NT_Q ? NT : 0); SB(); }
    const u32 di = (lr & 7) >> 1, sh = (lr & 1) * 16;
    u32 se[4], so[4];
#pragma unroll
    for (int i = 0; i < 4; ++i) {
      se[i] = di == (u32)i ? ((0x0100u << sh) | (0x0c0c0000u >> sh)) : 0x0c0c0c0cu;
      so[i] = di == (u32)i ? ((0x0302u << sh) | (0x0c0c0000u >> sh)) : 0x0c0c0c0cu;
    }
#pragma unroll
    for (int s = 0; s < 18; ++s)
#pragma unroll
      for (int i = 0; i < 4; ++i) xa[s][i] = __builtin_amdgcn_perm(0u, xs[s / 6][(s % 6) >> 1], (s & 1) ? so[i] : se[i]);
    u32 qo = WS_QP + (ha * 8 + jm) * 1024 + lane * 16;
    PIN(qo);
    const u32 tag = get_tag();
    SB();
    v4f acc = {0.f, 0.f, 0.f, 0.f};
#pragma unroll
    for (int s = 0; s < 18; ++s) acc = mfma(xa[s], wqv[s], acc);
    __builtin_amdgcn_raw_buffer_store_b128(tagged(acc, tag), rws, qo, 0, SC1);
    __builtin_amdgcn_s_setprio(0);
  } else if (g2role) {
    if (w >= 4) {
      const v4u suk = mk_srd(wuk, 512 * 8192);
      wait_until(mine + KNOB(4, K_TUK));
#pragma unroll
      for (int t = 0; t < 8; ++t) (K_NT_Q ? dman : dmac)(suk, ukv, (128 * j + 16 * t) * 8192 + h * 256, L2_UK + t * 4096 + (w & 3) * 1024);
    } else {
      if (w == 1 && blk < 8 && lane == 0) __builtin_amdgcn_raw_buffer_store_b32(mine, rws, WS_T0 + blk * 128, 0, SC1);
    }
  } else {

  }
  STAMP(1);
  const u32 q4 = (lane >> 2) & 3, p4 = lane & 3;
  const u32 fr0 = 4 * (lg & 1) + q4, fr1 = fr0 ^ 4, fpc = 2 * (w & 3) + (p4 >> 1);
  const u32 fa0 = fr0 * 128 + ((fpc ^ (fr0 & 6)) << 4) + 8 * (p4 & 1), fa1 = fr1 * 128 + ((fpc ^ (fr1 & 6)) << 4) + 8 * (p4 & 1);
  const u32 rr = lane >> 3, pcs = ((lane & 7) ^ (rr & 6)) << 4;
  auto frag = [&](u32 a) { return cat(ldstr(L, a + fa0), ldstr(L, a + fa1)); };
  auto small_dma = [&](u32 lx, u32 lgw) {
    const v4u sx = mk_srd((u64)x, 2304), sg = mk_srd(gw, 1152);
    dmac(sg, lane * 16, 0, lgw);
    dmac(sx, lane * 16, 0, lx); dmac(sx, lane * 16, 1024, lx + 1024);
    if (lane < 16) dmac(sx, lane * 16, 2048, lx + 2048);
    if (lane < 8) dmac(sg, lane * 16, 1024, lgw + 1024);
  };
  auto gates = [&](u32 lx, u32 lgw) {
    float a = 0.f;
#pragma unroll
    for (int i = 0; i < 9; ++i) {
      const u32 xv = *(LAS u32*)(L + lx + (lane >> 5) * 1152 + ((lane & 31) + 32 * i) * 4), gv = *(LAS u32*)(L + lgw + ((lane & 31) + 32 * i) * 4);
      a += bflo(xv) * bflo(gv) + bfhi(xv) * bfhi(gv);
    }
    return 1.f / (1.f + __expf(-add16(sum16(a))));
  };
  auto w_dma = [&](v4u srd, u32 pitch, u32 base, u32 lds, u32 first, u32 step) {
#pragma nounroll
    for (u32 slot = first; slot < 45; slot += step) {
      const u32 rbl = (slot * 57) >> 9, cg = slot - 9 * rbl;
      dma(srd, (slot >= 36 && rr >= 4 ? 0x80000000u : rr * pitch) + pcs, base + 8 * rbl * pitch + cg * 128, lds + slot * 1024);
    }
  };
  u32 gA0 = fa0 + (lg * 9 + (w >> 2)) * 1024, gA1 = fa1 + (lg * 9 + (w >> 2)) * 1024, gB0 = fa0 + (36 + (w >> 2)) * 1024, gB1 = fa1 + (36 + (w >> 2)) * 1024;
  u32 gC0 = fa0 + (lg * 9 + 8) * 1024, gC1 = fa1 + (lg * 9 + 8) * 1024, gH = (lr & 1) * 128 + 16 * lg;
  PIN(gA0); PIN(gA1); PIN(gB0); PIN(gB1); PIN(gC0); PIN(gC1); PIN(gH);
    auto gemv36 = [&](u32 lw, u32 lb, u32 vo, u32 stride, u32 tag) {
      const v4u h0 = lds128(L + lb, gH), h1 = lds128(L + lb + 64, gH);
      v4u a0[5], a1[5];
#pragma unroll
      for (int n = 0; n < 4; ++n) {
        a0[n] = cat(ldstr(L + lw + n * 2048, gA0), ldstr(L + lw + n * 2048, gA1));
        a1[n] = cat(ldstr(L + lw + n * 2048, gB0), ldstr(L + lw + n * 2048, gB1));
      }
      a0[4] = cat(ldstr(L + lw, gC0), ldstr(L + lw, gC1));
      a1[4] = cat(ldstr(L + lw + 44 * 1024, fa0), ldstr(L + lw + 44 * 1024, fa1));
      SB();
      v4f acc[5];
#pragma unroll
      for (int n = 0; n < 5; ++n) acc[n] = mfma(h1, a1[n], mfma(h0, a0[n], v4f{0.f, 0.f, 0.f, 0.f}));
      __builtin_amdgcn_raw_buffer_store_b64(v2u{sel4(acc[0], acc[1], acc[2], acc[3], lg), tag}, rws, vo, 0, SC1);
      if (w < 4 && lane < 16) __builtin_amdgcn_raw_buffer_store_b64(v2u{pk(acc[4][0], acc[4][1]), tag}, rws, vo, 32 * stride, SC1);
    };

  if (g2role) {
    const bool reducer = k < 9;
    const u32 rho = 9 * xc + k, rmt = rho >> 1, rb_ = rho & 1;
    u32 aqp = WS_QP + (h * 8 + w) * 1024 + lane * 16, abq = L2_BQ + ((lg >> 1) * 8 + w) * 272 + (8 * lr + 4 * (lg & 1)) * 2;
    u32 auk = L2_UK + w * 4096 + lr * 256, abr = lr * 272 + 16 * lg, aqg = WS_QG + ((lg >> 1) * 32 + h) * 2048 + (32 * j + 4 * w + (lr >> 2)) * 16;
    u32 auk0 = auk + ((lg ^ lr) << 4), auk1 = auk + (((4 + lg) ^ lr) << 4), auk2 = auk + (((8 + lg) ^ lr) << 4), auk3 = auk + (((12 + lg) ^ lr) << 4);
    PIN(aqp); PIN(abq); PIN(abr); PIN(aqg); PIN(auk0); PIN(auk1); PIN(auk2); PIN(auk3);
    u32 mli = WS_ML + (2 * (8 * w + lg) + (h >> 4)) * 256 + (h & 15) * 16;
    u32 ogo = WS_OG + h * OG_H + (j * 64 + 8 * w + lg) * 256 + lr * 16;
    u32 cki = WS_CK + (2 * (8 * w + lg) + (h >> 4)) * 512 + j * 128 + (h & 15) * 8 + (lr >> 3) * 4, cf[4];
#pragma unroll
    for (int i = 0; i < 4; ++i) { cf[i] = coef(4 * (lr & 7) + i); PIN(cf[i]); }
    PIN(cki);
    const u32 U1 = 2 * lr + (lg & 1);
    u32 abc = L2_BC + ((w >> 2) * 8 + (w & 3) * 2 + (lg >> 1)) * 272 + (U1 ^ ((U1 >> 1) & 1)) * 8;
    u32 ail = L2_F + (lg >> 1) * 32;
    u32 cgo = WS_CG + ((h * 4 + (w >> 1)) * 4 + j) * 256 + (w & 1) * 128 + lr * 8;
    u32 fro = WS_FG + rmt * 16384 + (tid >> 2) * 128 + rb_ * 64 + (tid & 3) * 16;
    u32 sel[4];
    {
      const u32 di = (lr & 7) >> 1, sh = (lr & 1) * 16, src = (lr >> 3) ? 0x0302u : 0x0100u;
#pragma unroll
      for (int i = 0; i < 4; ++i) { sel[i] = di == (u32)i ? ((src << sh) | (0x0c0c0000u >> sh)) : 0x0c0c0c0cu; PIN(sel[i]); }
    }
    u32 abr2 = L2_BQ + abr, aml = L2_F + (w >> 2) * 32 + (w & 3) * 4, ard = L2_RD + (lr * 4 + 2 * (lg & 1) + (lg >> 1)) * 32 + w * 4, oout = (((tid >> 3) & 1) * 576 + 8 * rho + (tid >> 4)) * 2;
    PIN(abr2); PIN(aml); PIN(ard); PIN(oout);
    PIN(mli); PIN(ogo); PIN(abc); PIN(cgo); PIN(ail); PIN(fro);
    u32 hgo = WS_HG + g1 * 4608 + (w + 8 * lg) * 128 + lr * 8;
    PIN(hgo);
    const u32 tag = get_tag();
    wait_until(mine + KNOB(3, K_T1A));
    v4u qp;
    u32 st0;
    asm volatile("buffer_load_dword %0, %2, %4, 0 offen sc1\n\tbuffer_load_dwordx4 %1, %3, %4, 0 offen sc1"
                 : "=&v"(st0), "=&v"(qp) : "v"(w == 1 ? WS_T0 + (lane & 7) * 128 : 0x80000000u), "v"(aqp), "s"(mk_srd(ws, WS_END)) : "memory");
    const v4u sw1 = mk_srd(w1, 576 * 9216);
    if (K_W1N) RAWBAR();
#pragma unroll
    for (int n = 0; n < K_W1N; ++n) {
      const u32 slot = w + 8 * n, rbl = (slot * 57) >> 9;
      dma(sw1, rr * 9216 + pcs, (36 * k + 8 * rbl) * 9216 + xc * 1152 + (slot - 9 * rbl) * 128, L2_W1 + slot * 1024);
    }
    asm volatile("s_waitcnt vmcnt(%2)" : "+v"(st0), "+v"(qp) : "i"(K_W1N) : "memory");
    if (__builtin_expect(!allok(bad(qp, tag)), 0)) {
#pragma nounroll
      for (u32 sp = 0; sp < SPIN_CAP; ++sp) {
        ORDER();
        qp = ld16(rws, aqp);
        COUNT(0);
        if (allok(bad(qp, tag))) break;
      }
    }
    *(LAS v2u*)(L + abq) = v2u{qp.x, qp.z};
    if (w == 1) *(LAS u32*)(L + L2_IL + 8) = latest_start(st0, mine);
    STAMP(2);
    BAR();
    {
      const v4u a0 = lds128(L, auk0), b0 = lds128(L, abr2), a1 = lds128(L, auk1), b1_ = lds128(L + 0 + 64, abr2);
      const v4u a2 = lds128(L, auk2), b2_ = lds128(L + 0 + 128, abr2), a3 = lds128(L, auk3), b3 = lds128(L + 0 + 192, abr2);
      SB();
      v4f acc = {0.f, 0.f, 0.f, 0.f};
      acc = mfma(b0, a0, acc); acc = mfma(b1_, a1, acc); acc = mfma(b2_, a2, acc); acc = mfma(b3, a3, acc);
      const float sc = 0.08838834764831845f * 1.4426950408889634f;
      const float v = add16((acc[0] + acc[1]) + (acc[2] + acc[3])) * sc;
      const u32 p01 = pk(v, dpp<0xB1>(v));
      const u32 p23 = __builtin_amdgcn_update_dpp(0, (int)p01, 0x4E, 0xf, 0xf, true);
      if ((lane & 19) == 0) __builtin_amdgcn_raw_buffer_store_b128(v4u{p01, tag, p23, tag}, rws, aqg, 0, SC1);
    }
    STAMP(3);
    RAWBAR();
    const u32 t0 = __builtin_amdgcn_readfirstlane(*(LAS u32*)(L + L2_IL + 8));
    const v4u suv = mk_srd(wuv, 512 * 8192);
    wait_until(t0 + KNOB(9, K_TS3));
    if (w) {
      w_dma(sw1, 9216, 36 * k * 9216 + xc * 1152, L2_W1, 8 * K_W1N + w - 1, 7);
#pragma nounroll
      for (u32 slot = w - 1; slot < 32; slot += 7)
        dma(suv, (16 * (rr & 3) + (rr >> 2)) * 8192 + pcs, (128 * j + 64 * (slot >> 4) + (slot & 14)) * 8192 + h * 256 + (slot & 1) * 128, L2_UV + slot * 1024);
      VMW(4);
    } else {
      small_dma(L2_X, L2_GW);
      if (lane < 4) dma4(mk_srd((u64)b2, 1152), lane * 4, 16 * rho, L2_B2);
      VMW(0);
#pragma unroll
      for (int i = 0; i < 2; ++i) {
        const u32 U = lane >> 2, xv = *(LAS u16*)(L + L2_X + i * 1152 + 72 * k + lane * 2);
        *(LAS u16*)(L + L2_XB + i * 128 + (U ^ ((U >> 1) & 1)) * 8 + (lane & 3) * 2) = lane < 36 ? (u16)xv : (u16)0;
      }
      const float gate = gates(L2_X, L2_GW);
      if (!(lane & 31)) *(LAS float*)(L + L2_IL + 16 + (lane >> 5) * 4) = gate;
    }
    BAR();
    STAMP(4);
    gemv36(L2_W1, L2_XB, hgo, 128, tag);
    STAMP(5);
    if (w < 4) { VMW(2); } else { VMW(1); }
    STAMP(12);
    BAR();
    v4u uvA[4];
#pragma unroll
    for (int s = 0; s < 4; ++s) { uvA[s] = frag(L2_UV + ((4 * s + lg) * 2 + (w >> 2)) * 1024); PIN(uvA[s]); }
    float gate = *(LAS float*)(L + L2_IL + 16 + (lg >> 1) * 4);
    PIN(gate);
    wait_until(t0 + KNOB(5, K_TM));
    STAMP(6);
    v4u ml[2], og[2];
    u32 ck0, ck1;
    auto og_load = [&]() {
      ml[0] = ld16(rws, mli); ml[1] = ld16(rws, mli + 2048);
      ck0 = ld4(rws, cki); ck1 = ld4(rws, cki + 4096);
      SB();
      og[0] = ld16(rws, ogo); og[1] = ld16(rws, ogo + 1024);
      SB();
    };
    v4u fA[2];
    float Mw, Lw;
    auto weights = [&]() {
      Mw = max32(max16(fmaxf(f_(ml[0].x), f_(ml[1].x))));
      const float f0 = __builtin_amdgcn_exp2f(f_(ml[0].x) - Mw), f1 = __builtin_amdgcn_exp2f(f_(ml[1].x) - Mw);
      const u32 p = pk(f0, f1), v0 = pk(f0, f0 - bflo(p)), v1 = pk(f1, f1 - bfhi(p));
#pragma unroll
      for (int i = 0; i < 4; ++i) { fA[0][i] = __builtin_amdgcn_perm(0u, v0, sel[i]); fA[1][i] = __builtin_amdgcn_perm(0u, v1, sel[i]); }
      Lw = add32(add16(f0 * f_(ml[0].z) + f1 * f_(ml[1].z)));
      PIN(fA[0]); PIN(fA[1]); PIN(Lw);
    };
    og_load();
    weights();
    SB();
    auto og_ok = [&]() {
      const u32 s0 = usum8(hdot(og[0].x, cf[0], hdot(og[0].y, cf[1], hdot(og[0].z, cf[2], hdot(og[0].w, cf[3], 0u)))));
      const u32 s1 = usum8(hdot(og[1].x, cf[0], hdot(og[1].y, cf[1], hdot(og[1].z, cf[2], hdot(og[1].w, cf[3], 0u)))));
      return allok((s0 + tag - ck0) | (s1 + tag - ck1) | bad(ml[0], tag) | bad(ml[1], tag));
    };
    if (__builtin_expect(!og_ok(), 0)) {
#pragma nounroll
      for (u32 sp = 0; sp < SPIN_CAP; ++sp) {
        COUNT(2);
        ORDER();
        og_load();
        if (og_ok()) break;
      }
      weights();
    }
    STAMP(7);
    {
      v4f acc = {0.f, 0.f, 0.f, 0.f};
      acc = mfma(fA[0], og[0], acc);
      acc = mfma(fA[1], og[1], acc);
      *(LAS v2u*)(L + abc) = v2u{pk(acc[0], acc[1]), pk(acc[2], acc[3])};
      *(LAS float*)(L + aml) = Mw; *(LAS float*)(L + aml + 16) = Lw;
    }
    BAR();
    {
      const v4u c0 = lds128(L + 4352, abr2), c1 = lds128(L + 4416, abr2), c2 = lds128(L + 4480, abr2), c3 = lds128(L + 4544, abr2);
      const v4f m4 = *(LAS v4f*)(L + ail), l4 = *(LAS v4f*)(L + ail + 16);
      SB();
      v4f acc = {0.f, 0.f, 0.f, 0.f};
      acc = mfma(c0, uvA[0], acc); acc = mfma(c1, uvA[1], acc); acc = mfma(c2, uvA[2], acc); acc = mfma(c3, uvA[3], acc);
      const float Mb = fmaxf(fmaxf(m4[0], m4[1]), fmaxf(m4[2], m4[3]));
      const float e0 = __builtin_amdgcn_exp2f(m4[0] - Mb), e1 = __builtin_amdgcn_exp2f(m4[1] - Mb), e2 = __builtin_amdgcn_exp2f(m4[2] - Mb), e3 = __builtin_amdgcn_exp2f(m4[3] - Mb);
      const float il = gate * __builtin_amdgcn_rcpf(e0 * l4[0] + e1 * l4[1] + e2 * l4[2] + e3 * l4[3]);
      const float v = add16((acc[0] + acc[1]) * (lg & 1 ? e2 : e0) + (acc[2] + acc[3]) * (lg & 1 ? e3 : e1)) * il;
      const auto bb = __builtin_amdgcn_permlane32_swap(u_(v), u_(v), false, false);
      if (lane < 16) __builtin_amdgcn_raw_buffer_store_b64(v2u{pk(f_(bb[0]), f_(bb[1])), tag}, rws, cgo, 0, SC1);
    }
    STAMP(8);
    if (reducer) {
      float fin = (1.f - *(LAS float*)(L + L2_IL + 16 + ((tid >> 3) & 1) * 4)) * bflo(*(LAS u16*)(L + L2_B2 + ((tid >> 4) & 7) * 2));
      PIN(fin);
      wait_until(t0 + KNOB(7, K_TF));
      v4u pa = ld16(rws, fro);
      if (__builtin_expect(!allok(bad(pa, tag)), 0)) {
#pragma nounroll
        for (u32 sp = 0; sp < SPIN_CAP; ++sp) {
          COUNT(3);
          ORDER();
          pa = ld16(rws, fro);
          if (allok(bad(pa, tag))) break;
        }
      }
      STAMP(10);
      const v4f v = {bflo(pa.x), bfhi(pa.x), bflo(pa.z), bfhi(pa.z)};
      const auto s01 = __builtin_amdgcn_permlane32_swap(u_(v[0]), u_(v[1]), false, false), s23 = __builtin_amdgcn_permlane32_swap(u_(v[2]), u_(v[3]), false, false);
      const auto sq = __builtin_amdgcn_permlane16_swap(u_(f_(s01[0]) + f_(s01[1])), u_(f_(s23[0]) + f_(s23[1])), false, false);
      float t = f_(sq[0]) + f_(sq[1]);
      t += dpp<0x124>(t); t += dpp<0x128>(t);
      if (lr < 4) *(LAS float*)(L + ard) = t;
      BAR();
      if (tid < 128) {
        const float s = sum8(*(LAS float*)(L + L2_RD + tid * 4)) + fin;
        if (!(tid & 7)) *(GAS u16*)((GAS char*)out + oout) = (u16)pk(s, 0.f);
      }
      if (blk == 0 && tid == 0) __hip_atomic_store((GAS u32*)(ws + WS_E), tag, __ATOMIC_RELAXED, __HIP_MEMORY_SCOPE_AGENT);
      STAMP(11);
    }
  } else {
    const u32 hu = tid >> 4, hf = 2 * hu + ((lr >> 1) & 1);
    const u32 sg = (0x9cu >> (2 * lg)) & 3;
    const u32 pir = ((lr & 3) << 2) | (lr >> 2);
    u32 qo = WS_QG + (b * 32 + hg * 16) * 2048 + tid * 16;
    u32 akr[4], aqr[4], aqw[4];
#pragma unroll
    for (int s = 0; s < 4; ++s) {
      akr[s] = (16 * w + lr) * 1024 + (((4 * s + sg) ^ pir) << 4);
      aqr[s] = L1_Q + lr * 1024 + (((4 * s + sg) ^ lr) << 4);
      const u32 head = 4 * s + (w >> 1), c4 = tid & 127;
      aqw[s] = L1_Q + head * 1024 + (((c4 >> 1) ^ head) << 4) + (c4 & 1) * 8;
      PIN(akr[s]); PIN(aqr[s]); PIN(aqw[s]);
    }
    u32 amx = L1_MX + lr * 32, apw = L1_P + lr * 272 + (16 * w + 4 * lg) * 2, apr = L1_P + lr * 272 + 16 * lg;
    u32 oo = WS_OG + (hg * 16 + 4 * lg) * OG_H + ((w >> 1) * 64 + tile) * 256 + (w & 1) * 128 + lr * 8, sto = WS_ML + pidx * 256 + lr * 16;
    PIN(qo); PIN(amx); PIN(apw); PIN(apr); PIN(oo); PIN(sto);
    u32 c0 = coef(2 * lr), c1 = coef(2 * lr + 1), cko = WS_CK + pidx * 512 + (w >> 1) * 128 + (4 * lg + lr) * 8 + (w & 1) * 4;
    PIN(c0); PIN(c1); PIN(cko);
    u32 avt[4][2];
#pragma unroll
    for (int tt = 0; tt < 4; ++tt) {
      avt[tt][0] = (8 * lg + q4) * 1024 + (((8 * w + 2 * tt + (p4 >> 1)) ^ ((q4 << 2) | (2 * (lg & 1)))) << 4) + 8 * (p4 & 1);
      avt[tt][1] = avt[tt][0] ^ 16;
      PIN(avt[tt][0]); PIN(avt[tt][1]);
    }
    if (tid < 96) *(LAS u32*)(L + L1_HB + tid * 4) = 0;
    if (w) wait_until(mine + KNOB(1, K_TP));
    u32 st0 = 0;
    if (w == 1) {
      dma4(mk_srd((u64)b1, 9216), lane * 4, 72 * g1, L1_B1);
      small_dma(L1_X, L1_GW);
      asm volatile("buffer_load_dword %0, %1, %2, 0 offen sc1" : "+v"(st0) : "v"(WS_T0 + (lane & 7) * 128), "s"(mk_srd(ws, WS_END)) : "memory");
    }
    if (w) {
      ck_dma(0, 3);
#pragma nounroll
      for (u32 i = 3; i < KNOB(0, K_EARLY); ++i) { VMW(2); ck_dma(i, i + 1); }
    }
    RAWBAR();
    ck_dma(w ? KNOB(0, K_EARLY) : 0, 8);
    if (w == 1) {
      asm volatile("s_waitcnt vmcnt(8)" : "+v"(st0)::"memory");
      *(LAS u32*)(L + L1_T0) = latest_start(st0, mine);
      const float gt = gates(L1_X, L1_GW);
      if (!(lane & 31)) *(LAS float*)(L + L1_GT + (lane >> 5) * 4) = 1.f - gt;
    }
    ck_dma(8, 16);
    STAMP(2);
    const u32 tag = get_tag();
    u32 t0 = __builtin_amdgcn_readfirstlane(*(volatile LAS u32*)(L + L1_T0));
    t0 = start_ok(t0, mine) ? t0 : mine;
    wait_until(t0 + KNOB(10, K_TQ));
    v4u qg[4];
#pragma unroll
    for (int u = 0; u < 4; ++u) qg[u] = ld16(rws, qo + u * 8192);
    VMW(4);
    STAMP(3);
    v4u kA[16];
#pragma unroll
    for (int s = 0; s < 16; ++s) kA[s] = lds128(L, akr[s & 3] + (s >> 2) * 256);
    BAR();
    v4u vA[4][4];
#pragma unroll
    for (int tt = 0; tt < 4; ++tt) {
      vA[tt][0] = cat(ldstr(L, avt[tt][0]), ldstr(L + 4096, avt[tt][1]));
      vA[tt][1] = cat(ldstr(L + 32768, avt[tt][0]), ldstr(L + 36864, avt[tt][1]));
    }
#pragma unroll
    for (int tt = 0; tt < 4; ++tt) {
      const u32 a0 = avt[tt][0] + 65536, a1 = avt[tt][1] + 65536;
      vA[tt][2] = cat(ldstr(L, a0), ldstr(L + 4096, a1));
      vA[tt][3] = cat(ldstr(L + 32768, a0), ldstr(L + 36864, a1));
    }
    const v4u sw2 = mk_srd(w2, 4608 * 1152);
    u32 w2v = rr * 1152 + pcs, w2s[4];
#pragma unroll
    for (int n = 0; n < K_W2N; ++n) {
      const u32 slot = w + 8 * n, rbl = (slot * 57) >> 9;
      w2s[n] = (36 * g1 + 8 * rbl) * 1152 + (slot - 9 * rbl) * 128;
      asm volatile("" : "+s"(w2s[n]));
    }
    PIN(w2v);
    ORDER();
    if (__builtin_expect(!allok(bad(qg[0], tag) | bad(qg[1], tag) | bad(qg[2], tag) | bad(qg[3], tag)), 0)) {
#pragma nounroll
      for (u32 sp = 0; sp < SPIN_CAP; ++sp) {
        COUNT(0);
        ORDER();
#pragma unroll
        for (int u = 0; u < 4; ++u)
          if (!allok(bad(qg[u], tag))) qg[u] = ld16(rws, qo + u * 8192);
        if (allok(bad(qg[0], tag) | bad(qg[1], tag) | bad(qg[2], tag) | bad(qg[3], tag))) break;
      }
    }
#pragma unroll
    for (int u = 0; u < 4; ++u) *(LAS v2u*)(L + aqw[u]) = v2u{qg[u].x, qg[u].z};
    STAMP(4);
    BAR();
    v4f S = {0.f, 0.f, 0.f, 0.f};
    {
      v4u qB[16];
#pragma unroll
      for (int s = 0; s < 16; ++s) qB[s] = lds128(L, aqr[s & 3] + (s >> 2) * 256);
      SB();
#pragma unroll
      for (int s = 0; s < 16; ++s) {
        S = mfma(kA[s], qB[s], S);
        if ((s & 3) == 1 && (s >> 2) < K_W2N) { dma(sw2, w2v, w2s[s >> 2], L1_W2 + (w + 2 * (s & 12)) * 1024); SB(); }
      }
    }
    {
      const float mw = max32(max16(fmaxf(fmaxf(S[0], S[1]), fmaxf(S[2], S[3]))));
      if (lane < 16) *(LAS float*)(L + amx + w * 4) = mw;
    }
    BAR();
    STAMP(5);
    float M;
    {
      const v4f m0 = *(LAS v4f*)(L + amx), m1 = *(LAS v4f*)(L + amx + 16);
      M = fmaxf(fmaxf(fmaxf(m0[0], m0[1]), fmaxf(m0[2], m0[3])), fmaxf(fmaxf(m1[0], m1[1]), fmaxf(m1[2], m1[3])));
      const float p0 = __builtin_amdgcn_exp2f(S[0] - M), p1 = __builtin_amdgcn_exp2f(S[1] - M), p2 = __builtin_amdgcn_exp2f(S[2] - M), p3 = __builtin_amdgcn_exp2f(S[3] - M);
      *(LAS v2u*)(L + apw) = v2u{pk(p0, p1), pk(p2, p3)};

    }
    BAR();
    {
      v4u pB[4];
#pragma unroll
      for (int t = 0; t < 4; ++t) pB[t] = lds128(L, apr + 64 * t);
      SB();
      if (w == 3) {
        v4f acc = {0.f, 0.f, 0.f, 0.f};
        const v4u ones = {0x3f803f80u, 0x3f803f80u, 0x3f803f80u, 0x3f803f80u};
#pragma unroll
        for (int t = 0; t < 4; ++t) acc = mfma(ones, pB[t], acc);
        if (lane < 16) __builtin_amdgcn_raw_buffer_store_b128(v4u{u_(M), tag, u_(acc[0]), tag}, rws, sto, 0, SC1);
      }
      v4f o[4];
#pragma unroll
      for (int tt = 0; tt < 4; ++tt) o[tt] = v4f{0.f, 0.f, 0.f, 0.f};
#pragma unroll
      for (int t = 0; t < 4; ++t)
#pragma unroll
        for (int tt = 0; tt < 4; ++tt) o[tt] = mfma(pB[t], vA[tt][t], o[tt]);
      u32 hs[4];
#pragma unroll
      for (int r = 0; r < 4; ++r) {
        const u32 d0 = pk(o[0][r], o[1][r]), d1 = pk(o[2][r], o[3][r]);
        __builtin_amdgcn_raw_buffer_store_b64(v2u{d0, d1}, rws, oo, r * OG_H, SC1);
        hs[r] = hdot(d0, c0, hdot(d1, c1, 0u));
      }
      SB();
      const bool o1 = lr & 1, o2 = lr & 2;
      const u32 h01 = (o1 ? hs[1] : hs[0]) + dppu<0xB1>(o1 ? hs[0] : hs[1]), h23 = (o1 ? hs[3] : hs[2]) + dppu<0xB1>(o1 ? hs[2] : hs[3]);
      u32 hh = (o2 ? h23 : h01) + dppu<0x4E>(o2 ? h01 : h23);
      hh += dppu<0x124>(hh); hh += dppu<0x128>(hh);
      if (lr < 4) __builtin_amdgcn_raw_buffer_store_b32(hh + tag, rws, cko, 0, SC1);
    }
    RAWBAR();
    STAMP(6);
    const u32 ho = WS_HG + (16 * xc + lr) * 4608 + (18 * k + min(hu, 17u)) * 16;
    w_dma(sw2, 1152, 36 * g1 * 1152, L1_W2, w + 8 * K_W2N, 8);
    {
      const v4u swo = mk_srd(wo, 4096 * 1152);
#pragma nounroll
      for (u32 slot = 7 - w; slot < 36; slot += 8) {
        const u32 rbl = (slot * 57) >> 9, cg = slot - 9 * rbl;
        dma(swo, rr * 1152 + pcs, (32 * g1 + 8 * rbl) * 1152 + cg * 128, L1_WO + slot * 1024);
      }
    }
    wait_until(t0 + KNOB(8, K_TH));
    if (w < 5) {
      v4u hp = ld16(rws, ho);
      if (__builtin_expect(!allok(bad(hp, tag)), 0)) {
#pragma nounroll
        for (u32 sp = 0; sp < SPIN_CAP; ++sp) {
          ORDER();
          hp = ld16(rws, ho);
          COUNT(1);
          if (allok(bad(hp, tag))) break;
        }
      }
      STAMP(7);
      const float s0 = sum16(bflo(hp.x)), s1 = sum16(bfhi(hp.x)), s2 = sum16(bflo(hp.z)), s3 = sum16(bfhi(hp.z));
      const float v = (lr == 0 ? s0 : lr == 1 ? s1 : lr == 2 ? s2 : s3) + bflo(*(LAS u16*)(L + L1_B1 + hf * 2));
      const float u2 = 1.5957691216057308f * (v + 0.044715f * v * v * v);
      const float hv = v / (1.f + __expf(-u2)) * *(LAS float*)(L + L1_GT + (lr & 1) * 4);
      if (tid < 288 && lr < 4) *(LAS u16*)(L + L1_HB + (lr & 1) * 128 + ((hf >> 2) ^ ((hf >> 3) & 1)) * 8 + (hf & 3) * 2) = (u16)pk(hv, 0.f);
    }
    VMW(0);
    STAMP(9);
    BAR();
    v4f acc[5];
    v4u woA[5];
    {
      const u32 ah = lr & 6 ? 256 : gH;
      const v4u h0 = lds128(L + L1_HB, ah), h1 = lds128(L + L1_HB + 64, ah);
      v4u a0[5], a1[5];
#pragma unroll
      for (int n = 0; n < 4; ++n) {
        a0[n] = cat(ldstr(L + L1_W2 + n * 2048, gA0), ldstr(L + L1_W2 + n * 2048, gA1));
        a1[n] = cat(ldstr(L + L1_W2 + n * 2048, gB0), ldstr(L + L1_W2 + n * 2048, gB1));
      }
      a0[4] = cat(ldstr(L + L1_W2, gC0), ldstr(L + L1_W2, gC1));
      a1[4] = cat(ldstr(L + L1_W2 + 44 * 1024, fa0), ldstr(L + L1_W2 + 44 * 1024, fa1));
#pragma unroll
      for (int n = 0; n < 5; ++n) { acc[n] = mfma(h1, a1[n], mfma(h0, a0[n], v4f{0.f, 0.f, 0.f, 0.f})); PIN(acc[n]); }
    }
    const u32 U2 = lr >> 1;
    u32 cgi = WS_CG + g1 * 1024 + lane * 16, fo = WS_FG + g1 * 128 + lr * 8 + (w + 8 * lg) * 16384;
    u32 acw = L1_CW + w * 640 + lg * 160 + (U2 ^ ((U2 >> 1) & 1)) * 8 + (lr & 1) * 4, acr = L1_CW + w * 640 + (lr & 7) * 80 + lg * 16;
    PIN(cgi); PIN(fo); PIN(acw); PIN(acr);
#pragma unroll
    for (int n = 0; n < 4; ++n) woA[n] = cat(ldstr(L + L1_WO + n * 2048, gA0), ldstr(L + L1_WO + n * 2048, gA1));
    woA[4] = cat(ldstr(L + L1_WO, gC0), ldstr(L + L1_WO, gC1));
    STAMP(8);
    wait_until(t0 + KNOB(2, K_TC));
    v4u cg = ld16(rws, cgi);
    if (__builtin_expect(!allok(bad(cg, tag)), 0)) {
#pragma nounroll
      for (u32 sp = 0; sp < SPIN_CAP; ++sp) {
        ORDER();
        cg = ld16(rws, cgi);
        COUNT(2);
        if (allok(bad(cg, tag))) break;
      }
    }
    STAMP(11);
    {
      *(LAS u32*)(L + acw) = __builtin_amdgcn_perm(cg.z, cg.x, 0x05040100u);
      *(LAS u32*)(L + acw + 80) = __builtin_amdgcn_perm(cg.z, cg.x, 0x07060302u);
      const v4u ca = lds128(L, acr);
#pragma unroll
      for (int n = 0; n < 5; ++n) acc[n] = mfma(ca, woA[n], acc[n]);
      float o[2];
#pragma unroll
      for (int i = 0; i < 2; ++i) {
        const auto s01 = __builtin_amdgcn_permlane16_swap(u_(acc[0][i] + acc[0][i + 2]), u_(acc[1][i] + acc[1][i + 2]), false, false);
        const auto s23 = __builtin_amdgcn_permlane16_swap(u_(acc[2][i] + acc[2][i + 2]), u_(acc[3][i] + acc[3][i + 2]), false, false);
        o[i] = lg & 2 ? f_(s23[0]) + f_(s23[1]) : f_(s01[0]) + f_(s01[1]);
      }
      __builtin_amdgcn_raw_buffer_store_b64(v2u{pk(o[0], o[1]), tag}, rws, fo, 0, SC1);
      const u32 o4 = pk(add16(acc[4][0] + acc[4][2]), add16(acc[4][1] + acc[4][3]));
      if (w < 4 && lane < 16) __builtin_amdgcn_raw_buffer_store_b64(v2u{o4, tag}, rws, fo, 32 * 16384, SC1);
    }
    STAMP(10);
  }
  STAMP_OUT();
  asm volatile(".Lpf_end:");
}

extern "C" void kernel_launch(void* const* d_in, const int* in_sizes, int n_in, void* d_out, int out_size, void* d_ws, size_t ws_size, hipStream_t stream) {
  fused_decode<<<256, 512, 0, stream>>>((const u16*)d_in[0], (const u16*)d_in[1], (const u16*)d_in[2], (const u16*)d_in[3], (const u16*)d_in[4],
      (const u16*)d_in[5], (const u16*)d_in[7], (const u16*)d_in[8], (const u16*)d_in[9], (const u16*)d_in[10], (const u16*)d_in[11], (u16*)d_out, (char*)d_ws + WS_BASE(ws_size)
      );
}
```
